```python
import torch

def _softmax(x, axis=-1):
    return torch.softmax(x, dim=axis)
def _gelu(x):
    return torch.nn.functional.gelu(x, approximate="tanh")
def _silu(x):
    return torch.nn.functional.silu(x)
def _sigmoid(x):
    return torch.sigmoid(x)
def _layernorm(x, axis=-1, eps=1e-5):
    m = x.mean(dim=axis, keepdim=True)
    v = x.var(dim=axis, keepdim=True, unbiased=False)
    return (x - m) / torch.sqrt(v + eps)
def _scatter_add(tgt, idx, src, axis=0):
    return tgt.clone().index_add_(axis, idx, src)
def _segment_sum(src, seg, n):
    out = torch.zeros((n,) + src.shape[1:], dtype=src.dtype)
    return out.index_add_(0, seg, src)
def _pad(x, pw):
    flat = tuple(v for pair in reversed(pw) for v in pair)
    return torch.nn.functional.pad(x, flat)

kernel_name = 'concat_heads__mha_block__ssm_conv_s5__prompt'

def setup_inputs():

    _w = (lambda a: (lambda t: t.to(torch.bfloat16) if t.is_floating_point() else t)(torch.as_tensor(a)))
    def _A_setup():
        B, S, D, H = 1, 2048, 576, 32
        return {
            "x": _w((lambda *s: torch.randn(*s, dtype=torch.bfloat16) * 0.2)(B, S, D)),
            "wqkv": _w((lambda *s: torch.randn(*s, dtype=torch.bfloat16) * 0.2)(D, 3 * D)),
            "wo": _w((lambda *s: torch.randn(*s, dtype=torch.bfloat16) * 0.05)(D, D)),
        }
    _A_in = _A_setup()

    def _B_setup():
        B, S, D = 1, 2048, 576
        P = D
        Lambda_re = -0.5 * (lambda _x: torch.exp(torch.as_tensor(_x)))((lambda *s: torch.randn(*s, dtype=torch.bfloat16) * 0.8)(P))
        Lambda_im = (lambda *s: torch.randn(*s, dtype=torch.bfloat16) * 0.8)(P) * 1.0
        B_re = (lambda *s: torch.randn(*s, dtype=torch.bfloat16) * 0.8)(P, D) * (D ** -0.5)
        B_im = (lambda *s: torch.randn(*s, dtype=torch.bfloat16) * 0.8)(P, D) * (D ** -0.5)
        C_re = (lambda *s: torch.randn(*s, dtype=torch.bfloat16) * 0.8)(D, P) * (P ** -0.5)
        C_im = (lambda *s: torch.randn(*s, dtype=torch.bfloat16) * 0.8)(D, P) * (P ** -0.5)
        log_dt = (lambda *s: torch.randn(*s, dtype=torch.bfloat16) * 0.8)(P) * 0.3 - 2.0
        Dskip = (lambda *s: torch.randn(*s, dtype=torch.bfloat16) * 0.8)(D)
        return {
            "x": _w((lambda *s: torch.randn(*s, dtype=torch.bfloat16) * 0.05)(B, S, D)),
            "Lambda_re": _w(Lambda_re),
            "Lambda_im": _w(Lambda_im),
            "B_re": _w(B_re),
            "B_im": _w(B_im),
            "C_re": _w(C_re),
            "C_im": _w(C_im),
            "log_dt": _w(log_dt),
            "Dskip": _w(Dskip),
        }
    _B_in = _B_setup()

    return {
        "A_x": _A_in["x"],
        "A_wqkv": _A_in["wqkv"],
        "A_wo": _A_in["wo"],
        "B_x": _B_in["x"],
        "B_Lambda_re": _B_in["Lambda_re"],
        "B_Lambda_im": _B_in["Lambda_im"],
        "B_B_re": _B_in["B_re"],
        "B_B_im": _B_in["B_im"],
        "B_C_re": _B_in["C_re"],
        "B_C_im": _B_in["C_im"],
        "B_log_dt": _B_in["log_dt"],
        "B_Dskip": _B_in["Dskip"],
    }

def reference(A_x, A_wqkv, A_wo, B_x, B_Lambda_re, B_Lambda_im, B_B_re, B_B_im, B_C_re, B_C_im, B_log_dt, B_Dskip):
    def _A_ref(x, wqkv, wo):
        B, S, D = x.shape
        H = 32
        Dh = D // H
        qkv = (lambda x, *d: x.permute(*d))((x @ wqkv).reshape(B, S, 3, H, Dh), 2, 0, 3, 1, 4)
        q, k, v = qkv[0], qkv[1], qkv[2]
        scores = torch.einsum("bhqd,bhkd->bhqk", q, k) * (Dh ** -0.5)
        p = _softmax(scores, axis=-1)
        o = torch.einsum("bhqk,bhkd->bhqd", p, v)
        return (lambda x, *d: x.permute(*d))(o, 0, 2, 1, 3).reshape(B, S, D) @ wo

    def _B_ref(x, Lambda_re, Lambda_im, B_re, B_im, C_re, C_im, log_dt, Dskip):
        Bsz, L, H = x.shape
        Lam = Lambda_re + 1j * Lambda_im
        Bmat = B_re + 1j * B_im
        Cmat = C_re + 1j * C_im
        dt = torch.exp(log_dt)
        Lam_bar = torch.exp(dt * Lam)
        B_bar = ((Lam_bar - 1.0) / Lam)[:, None] * Bmat
        Bu = torch.einsum("bld,pd->blp", x + 0j, B_bar)
        h = torch.zeros_like(Bu[:, 0, :])
        hs = []
        for t in range(L):
            h = Lam_bar[None, :] * h + Bu[:, t, :]
            hs.append(h)
        hseq = torch.stack(hs, 1)
        y = 2.0 * torch.einsum("blp,dp->bld", hseq, Cmat).real + x * Dskip[None, None, :]
        return y

    a = _A_ref(A_x, A_wqkv, A_wo)
    b = _B_ref(B_x, B_Lambda_re, B_Lambda_im, B_B_re, B_B_im, B_C_re, B_C_im, B_log_dt, B_Dskip)
    return torch.concatenate([a, b], axis=-1)
```

```cpp
#include <hip/hip_runtime.h>
#include <hip/hip_bf16.h>

typedef unsigned short u16;
typedef __attribute__((ext_vector_type(8))) __bf16 bf16x8;
typedef __attribute__((ext_vector_type(16))) __bf16 bf16x16;
typedef __attribute__((ext_vector_type(2))) __bf16 bf16x2;
typedef __attribute__((ext_vector_type(4))) short s16x4;
typedef __attribute__((ext_vector_type(2))) float f32x2;
typedef __attribute__((ext_vector_type(8))) float f32x8;
typedef __attribute__((ext_vector_type(16))) float f32x16;
typedef __attribute__((ext_vector_type(32))) float f32x32;
typedef __attribute__((ext_vector_type(2))) unsigned u32x2;
typedef __attribute__((ext_vector_type(4))) unsigned u32x4;
typedef __attribute__((address_space(3))) const char* lds_cptr;
typedef __attribute__((address_space(3))) char* lds_ptr;

#define DI __device__ __forceinline__
#define SBAR() __builtin_amdgcn_sched_barrier(0)
#define PIN(x) asm volatile("" : "+v"(x))

constexpr int SEQ = 2048, DM = 576, NH = 32;

constexpr size_t WS_QM = 0;
constexpr size_t WS_QR = WS_QM + 32 * 65536;
constexpr size_t WS_KM = WS_QR + 16 * 16384;
constexpr size_t WS_KR = WS_KM + 32 * 65536;
constexpr size_t WS_V = WS_KR + 16 * 16384;
constexpr size_t WS_U = WS_V + 32 * 77824;
constexpr size_t WS_H = WS_U + (size_t)576 * 8192;
constexpr size_t WS_O = WS_H + (size_t)1152 * 4096;
constexpr size_t WS_TAB = WS_O + (size_t)64 * 36864;

DI unsigned pack_bf16(float lo, float hi) {
  f32x2 v = {lo, hi};
  return __builtin_bit_cast(unsigned, __builtin_convertvector(v, bf16x2));
}
DI float bf_lo(unsigned x) { return __uint_as_float(x << 16); }
DI float bf_hi(unsigned x) { return __uint_as_float(x & 0xffff0000u); }
DI float bf_val(u16 x) { return __uint_as_float((unsigned)x << 16); }

DI void dma16(const void* base, unsigned voff, unsigned lds_dst) {
  asm volatile("s_mov_b32 m0, %2\n\ts_nop 0\n\tglobal_load_lds_dwordx4 %0, %1" ::"v"(voff), "s"(base), "s"(lds_dst) : "memory");
}
#define WAIT_VM(n) asm volatile("s_waitcnt vmcnt(" #n ")" ::: "memory")
#define BARRIER() asm volatile("s_waitcnt lgkmcnt(0)\n\ts_barrier" ::: "memory")

DI bf16x8 lds_frag(lds_cptr p) { return *(const __attribute__((address_space(3))) bf16x8*)p; }
DI bf16x8 lds_frag_tr(lds_cptr p, int second) {
  typedef __attribute__((address_space(3))) s16x4* tp;
  const s16x4 lo = __builtin_amdgcn_ds_read_tr16_b64_v4i16((tp)p);
  const s16x4 hi = __builtin_amdgcn_ds_read_tr16_b64_v4i16((tp)(p + second));
  return __builtin_bit_cast(bf16x8, __builtin_shufflevector(lo, hi, 0, 1, 2, 3, 4, 5, 6, 7));
}
#define MFMA32(a, b, c) __builtin_amdgcn_mfma_f32_32x32x16_bf16((a), (b), (c), 0, 0, 0)
DI int crow(int reg, int h) { return (reg & 3) + 8 * (reg >> 2) + 4 * h; }

constexpr int P_BUF = 40960, P_XB = 16384;

template <int MODE>
DI void proj_compute(lds_cptr lds, int w, int lane, f32x16 (&acc)[2][3]) {
  const int r = lane & 31, h = lane >> 5, tw = w & 1, cw = w >> 1;
  int xa[4], wa[4];
#pragma unroll
  for (int ks = 0; ks < 4; ++ks) xa[ks] = (64 * tw + r) * 128 + (((2 * ks + h) ^ ((r >> 1) & 7)) << 4);
  if (MODE == 2) {
    const int row = (r & 1) * 96 + 48 * cw + (r >> 1);
    const int f = ((row >> 1) & 7) ^ (r & 1);
#pragma unroll
    for (int ks = 0; ks < 4; ++ks) wa[ks] = P_XB + row * 128 + (((2 * ks + h) ^ f) << 4);
  } else {
    const int blk = (lane >> 4) & 1, q = (lane & 15) >> 2, p = lane & 3;
#pragma unroll
    for (int j = 0; j < 3; ++j)
      wa[j] = P_XB + (8 * h + q) * 384 + (((12 * cw + 4 * j + 2 * blk + (p >> 1)) ^ ((q >> 1) << 2)) << 4) + 8 * (p & 1);
    wa[3] = 0;
  }
  bf16x8 xf[2][2], wf[2][3];
  auto load = [&](int s, int boff, int ks) {
#pragma unroll
    for (int i = 0; i < 2; ++i) xf[s][i] = lds_frag(lds + boff + xa[ks] + i * 4096);
#pragma unroll
    for (int j = 0; j < 3; ++j) {
      if (MODE == 2) wf[s][j] = lds_frag(lds + boff + wa[ks] + j * 2048);
      else wf[s][j] = lds_frag_tr(lds + boff + wa[j] + ks * 6144, 1536);
    }
  };
  auto mma = [&](int s) {
#pragma unroll
    for (int j = 0; j < 3; ++j)
#pragma unroll
      for (int i = 0; i < 2; ++i) {
        if (MODE == 1) acc[i][j] = MFMA32(xf[s][i], wf[s][j], acc[i][j]);
        else acc[i][j] = MFMA32(wf[s][j], xf[s][i], acc[i][j]);
      }
  };
  BARRIER();
  load(0, 0, 0);
  int boff = 0;
#pragma unroll 1
  for (int c = 0; c < 9; ++c) {
    load(1, boff, 1); SBAR(); mma(0); SBAR();
    load(0, boff, 2); SBAR(); mma(1); SBAR();
    load(1, boff, 3); SBAR(); mma(0); SBAR();
    boff = (boff == 3 * P_BUF) ? 0 : boff + P_BUF;
    if (c < 8) { BARRIER(); load(0, boff, 0); }
    SBAR(); mma(1); SBAR();
  }
}

DI void ssm_table(const u16* lre, const u16* lim, const u16* logdt, float* tab, int p) {
  const float dt = bf_val(__builtin_bit_cast(u16, (__bf16)expf(bf_val(logdt[p]))));
  const float lr = bf_val(lre[p]), li = bf_val(lim[p]);
  const float mag = expf(dt * lr);
  const float ang = dt * li;
  const float ar = mag * cosf(ang), ai = mag * sinf(ang);
  const double nr = (double)ar - 1.0, ni = ai, den = (double)lr * lr + (double)li * li;
  float* t = tab + p * 16;
  t[0] = ar; t[1] = ai;
  t[2] = (float)((nr * lr + ni * li) / den);
  t[3] = (float)((ni * lr - nr * li) / den);
  double pr = ar, pi = ai;
  for (int i = 0; i < 5; ++i) { const double n = pr * pr - pi * pi; pi = 2.0 * pr * pi; pr = n; }
  for (int i = 0; i < 6; ++i) {
    t[4 + 2 * i] = (float)pr; t[5 + 2 * i] = (float)pi;
    const double n = pr * pr - pi * pi; pi = 2.0 * pr * pi; pr = n;
  }
}

__global__ __launch_bounds__(512, 2) void k_proj(const u16* __restrict__ xa, const u16* __restrict__ wqkv,
                                                 const u16* __restrict__ xb, const u16* __restrict__ bre,
                                                 const u16* __restrict__ bim, const u16* __restrict__ lre,
                                                 const u16* __restrict__ lim, const u16* __restrict__ logdt,
                                                 char* __restrict__ ws) {
  extern __shared__ __attribute__((aligned(16))) char smem[];
  const int xcd = blockIdx.x & 7, slot = blockIdx.x >> 3;
  const int cb = (xcd & 1) * 8 + (slot >> 2), rt = (xcd >> 1) * 4 + (slot & 3);
  const int tid = threadIdx.x, lane = tid & 63;
  const int w = __builtin_amdgcn_readfirstlane(tid >> 6);
  if (cb == 15) {
    if (tid < 36) ssm_table(lre, lim, logdt, (float*)(ws + WS_TAB), rt * 36 + tid);
    return;
  }
  const unsigned lds0 = (unsigned)(size_t)(lds_ptr)smem;
  if (w >= 4) {
    const int lw = w - 4;
    unsigned xo[4], wo[6];
#pragma unroll
    for (int i = 0; i < 4; ++i) {
      const int row = 8 * (4 * lw + i) + (lane >> 3);
      xo[i] = row * 1152 + (((lane & 7) ^ ((row >> 1) & 7)) << 4);
    }
    const char *xs, *wsrc;
    unsigned wstep;
    if (cb < 9) {
      xs = (const char*)xa;
      wsrc = (const char*)wqkv + cb * 384;
      wstep = 64 * 3456;
#pragma unroll
      for (int i = 0; i < 6; ++i) {
        const int L = (6 * lw + i) * 64 + lane, k = L / 24, c = L % 24;
        wo[i] = k * 3456 + ((c ^ (((k >> 1) & 1) << 2)) << 4);
      }
    } else {
      xs = (const char*)xb;
      wsrc = (const char*)(lw < 2 ? bre : bim) + (cb - 9) * 96 * 1152;
      wstep = 128;
#pragma unroll
      for (int i = 0; i < 6; ++i) {
        const int row = 8 * (6 * (lw & 1) + i) + (lane >> 3);
        wo[i] = row * 1152 + (((lane & 7) ^ ((row >> 1) & 7) ^ (lw >> 1)) << 4);
      }
    }
    xs += rt * 128 * 1152;
    auto issue = [&](int c) {
      const unsigned b = lds0 + (c & 3) * P_BUF;
#pragma unroll
      for (int i = 0; i < 4; ++i) dma16(xs + c * 128, xo[i], b + (4 * lw + i) * 1024);
#pragma unroll
      for (int i = 0; i < 6; ++i) dma16(wsrc + (size_t)c * wstep, wo[i], b + P_XB + (6 * lw + i) * 1024);
    };
#pragma unroll 1
    for (int c = 0; c < 4; ++c) issue(c);
    WAIT_VM(30); BARRIER();
#pragma unroll 1
    for (int c = 1; c < 6; ++c) { WAIT_VM(20); BARRIER(); issue(c + 3); }
    WAIT_VM(20); BARRIER();
    WAIT_VM(10); BARRIER();
    WAIT_VM(0); BARRIER();
    return;
  }
  const int r = lane & 31, h = lane >> 5, tw = w & 1, cw = w >> 1;
  f32x16 acc[2][3];
#pragma unroll
  for (int i = 0; i < 2; ++i)
#pragma unroll
    for (int j = 0; j < 3; ++j) acc[i][j] = f32x16{};
  const int tile0 = rt * 4 + tw * 2;
  if (cb < 6) {
    proj_compute<0>((lds_cptr)smem, w, lane, acc);
    const bool isq = cb < 3;
    const float sc = isq ? 0.23570226039551584f * 1.4426950408889634f : 1.0f;
    const int sl = isq ? r : ((r & 19) | ((r & 4) << 1) | ((r & 8) >> 1));
    char* mainb = ws + (isq ? WS_QM : WS_KM);
    constexpr unsigned REM = WS_QR - WS_QM;
#pragma unroll
    for (int j = 0; j < 3; ++j)
#pragma unroll
      for (int m = 0; m < 4; ++m) {
        const int G = (isq ? cb : cb - 3) * 48 + cw * 24 + j * 8 + 2 * m + h;
        const int pair = G / 9, pos = G - 9 * pair;
        const int qd = pos > 4 ? pos - 5 : pos;
        const unsigned mo = (2 * pair + (pos > 4)) * 65536 + tile0 * 1024 + ((qd >> 1) * 32 + sl) * 16 + (qd & 1) * 8;
        const unsigned ro = REM + pair * 16384 + (tile0 * 32 + sl) * 8;
        const unsigned off = pos == 4 ? ro : mo, ts = pos == 4 ? 256 : 1024;
#pragma unroll
        for (int i = 0; i < 2; ++i) {
          const u32x2 v = {pack_bf16(acc[i][j][4 * m] * sc, acc[i][j][4 * m + 1] * sc),
                           pack_bf16(acc[i][j][4 * m + 2] * sc, acc[i][j][4 * m + 3] * sc)};
          *(u32x2*)(mainb + off + i * ts) = v;
        }
      }
  } else if (cb < 9) {
    proj_compute<1>((lds_cptr)smem, w, lane, acc);
#pragma unroll
    for (int j = 0; j < 3; ++j) {
      const int n = (cb - 6) * 192 + cw * 96 + j * 32 + r, head = n / 18, d = n - 18 * head;
      char* dst = ws + WS_V + head * 77824 + tile0 * 1216 + (h * 19 + d) * 16;
#pragma unroll
      for (int i = 0; i < 2; ++i)
#pragma unroll
        for (int s = 0; s < 2; ++s) {
          const f32x16& a = acc[i][j];
          const u32x4 v = {pack_bf16(a[8 * s], a[8 * s + 1]), pack_bf16(a[8 * s + 2], a[8 * s + 3]),
                           pack_bf16(a[8 * s + 4], a[8 * s + 5]), pack_bf16(a[8 * s + 6], a[8 * s + 7])};
          *(u32x4*)(dst + i * 1216 + s * 608) = v;
        }
      if (d == 17) {
        const u32x4 ones = {0x3f803f80u, 0x3f803f80u, 0x3f803f80u, 0x3f803f80u};
#pragma unroll
        for (int i = 0; i < 4; ++i) *(u32x4*)(dst + 16 + i * 608) = ones;
      }
    }
  } else {
    proj_compute<2>((lds_cptr)smem, w, lane, acc);
    char* dst = ws + WS_U + ((cb - 9) * 96 + cw * 48 + 2 * h) * 8192 + (tile0 * 32 + r) * 4;
#pragma unroll
    for (int j = 0; j < 3; ++j)
#pragma unroll
      for (int t = 0; t < 8; ++t)
#pragma unroll
        for (int i = 0; i < 2; ++i)
          *(unsigned*)(dst + (16 * j + 4 * (t >> 1) + (t & 1)) * 8192 + i * 128) = pack_bf16(acc[i][j][2 * t], acc[i][j][2 * t + 1]);
  }
}

constexpr int A_K = 0, A_V = 65536, A_R = A_V + 77824, A_END = A_R + 16384;

DI f32x2 cfma(f32x2 a, f32x2 x, f32x2 c) {
  return f32x2{__builtin_fmaf(a[0], x[0], __builtin_fmaf(-a[1], x[1], c[0])),
               __builtin_fmaf(a[0], x[1], __builtin_fmaf(a[1], x[0], c[1]))};
}

DI void ssm_scan(char* ws, int p, int lane, const u32x4 (&u)[8]) {
  const float* t = (const float*)(ws + WS_TAB) + p * 16;
  const f32x2 a = {t[0], t[1]};
  f32x2 e = {0.f, 0.f};
#pragma unroll
  for (int i = 0; i < 32; ++i) e = cfma(a, e, f32x2{bf_lo(u[i >> 2][i & 3]), bf_hi(u[i >> 2][i & 3])});
#pragma unroll
  for (int s = 0; s < 6; ++s) {
    const int d = 1 << s;
    f32x2 o = {__shfl_up(e[0], d), __shfl_up(e[1], d)};
    if (lane < d) o = f32x2{0.f, 0.f};
    e = cfma(f32x2{t[4 + 2 * s], t[5 + 2 * s]}, o, e);
  }
  f32x2 hs = {__shfl_up(e[0], 1), __shfl_up(e[1], 1)};
  if (lane == 0) hs = f32x2{0.f, 0.f};
  const float cr = 2.f * t[2], ci = 2.f * t[3];
  char* dre = ws + WS_H + (size_t)p * 4096 + lane * 64;
  char* dim = dre + (size_t)576 * 4096;
#pragma unroll
  for (int g = 0; g < 4; ++g) {
    float re[8], im[8];
#pragma unroll
    for (int i = 0; i < 8; ++i) {
      const unsigned x = u[2 * g + (i >> 2)][i & 3];
      hs = cfma(a, hs, f32x2{bf_lo(x), bf_hi(x)});
      re[i] = cr * hs[0] - ci * hs[1];
      im[i] = -cr * hs[1] - ci * hs[0];
    }
    *(u32x4*)(dre + g * 16) = u32x4{pack_bf16(re[0], re[1]), pack_bf16(re[2], re[3]), pack_bf16(re[4], re[5]), pack_bf16(re[6], re[7])};
    *(u32x4*)(dim + g * 16) = u32x4{pack_bf16(im[0], im[1]), pack_bf16(im[2], im[3]), pack_bf16(im[4], im[5]), pack_bf16(im[6], im[7])};
  }
}

DI f32x16 lo16(const f32x32& x) { return __builtin_shufflevector(x, x, 0, 1, 2, 3, 4, 5, 6, 7, 8, 9, 10, 11, 12, 13, 14, 15); }
DI f32x16 hi16(const f32x32& x) { return __builtin_shufflevector(x, x, 16, 17, 18, 19, 20, 21, 22, 23, 24, 25, 26, 27, 28, 29, 30, 31); }
DI bf16x16 p_lo(const f32x16& x) { return __builtin_bit_cast(bf16x16, __builtin_shufflevector(x, x, 0, 1, 2, 3, 4, 5, 6, 7)); }
DI bf16x16 p_hi(const f32x16& x) { return __builtin_bit_cast(bf16x16, __builtin_shufflevector(x, x, 8, 9, 10, 11, 12, 13, 14, 15)); }
#define PV(o, v, p) o = __builtin_amdgcn_smfmac_f32_32x32x32_bf16((v), (p), (o), sidx, 0, 0)
#define QKREM(kr, qr, blk) __builtin_amdgcn_mfma_f32_32x32x4bf16_1k((kr), (qr), f32x32{}, 1, (blk), 0)
#define EX(x, i) x[i] = __builtin_amdgcn_exp2f(x[i])

__global__ __launch_bounds__(512, 2) void k_attn(char* __restrict__ ws) {
  extern __shared__ __attribute__((aligned(16))) char smem[];
  const int b = blockIdx.x;
  const int head = 4 * (b & 7) + ((b >> 3) & 3), qb = b >> 5;
  const int tid = threadIdx.x, lane = tid & 63;
  const int w = __builtin_amdgcn_readfirstlane(tid >> 6);
  const int r = lane & 31, h = lane >> 5, qw = w & 3, kh = w >> 2;
  const unsigned lds0 = (unsigned)(size_t)(lds_ptr)smem;

  const bool scan = b < 192 && w < 3;
  const int chan = 3 * b + w;
  u32x4 u[8];
  if (scan) {
#pragma unroll
    for (int i = 0; i < 8; ++i) u[i] = *(const u32x4*)(ws + WS_U + (size_t)chan * 8192 + lane * 128 + i * 16);
  }
  const int qt = qb * 8 + qw * 2;
  const bf16x8 q0 = *(const bf16x8*)(ws + WS_QM + head * 65536 + qt * 1024 + lane * 16);
  const bf16x8 q1 = *(const bf16x8*)(ws + WS_QM + head * 65536 + qt * 1024 + 1024 + lane * 16);
  const unsigned qrw = *(const unsigned*)(ws + WS_QR + (head >> 1) * 16384 + (qt * 32 + lane) * 8 + (head & 1) * 4);
  {
    const unsigned vo = lane * 16;
#pragma unroll 1
    for (int i = w; i < 64; i += 8) dma16(ws + WS_KM + head * 65536 + i * 1024, vo, lds0 + A_K + i * 1024);
#pragma unroll 1
    for (int i = w; i < 76; i += 8) dma16(ws + WS_V + head * 77824 + i * 1024, vo, lds0 + A_V + i * 1024);
#pragma unroll 1
    for (int i = w; i < 16; i += 8) dma16(ws + WS_KR + (head >> 1) * 16384 + i * 1024, vo, lds0 + A_R + i * 1024);
  }
  if (scan) ssm_scan(ws, chan, lane, u);
  const u32x2 qru = (head & 1) ? u32x2{0u, qrw} : u32x2{qrw, 0u};
  const s16x4 qr = __builtin_bit_cast(s16x4, qru);
  WAIT_VM(0);
  BARRIER();

  const int vrow = r < 19 ? r : (r >= 20 && r < 28 ? 0 : 18);
  lds_cptr kp = (lds_cptr)smem + A_K + kh * 32768 + lane * 16;
  lds_cptr vp = (lds_cptr)smem + A_V + kh * 38912 + (h * 19 + vrow) * 16;
  lds_cptr rp = (lds_cptr)smem + A_R + kh * 8192 + lane * 8;
  typedef const __attribute__((address_space(3))) s16x4* rptr;
  int sidx = 0xDDDD;
  PIN(sidx);
  f32x16 o0 = {}, o1 = {};
  {
    f32x16 a0, a1, b0, b1;
    bf16x8 kf, va0, va1, vb0, vb1;
    s16x4 kr = *(rptr)rp;
    kf = lds_frag(kp);
    va0 = lds_frag(vp); va1 = lds_frag(vp + 608);
    { const f32x32 s = QKREM(kr, qr, 0); a0 = MFMA32(kf, q0, lo16(s)); a1 = MFMA32(kf, q1, hi16(s)); }
    vb1 = va1;
    b1 = f32x16{};
#define STEP(C0, C1, N0, N1, V0, V1, VN0, VP1, KOFF, VOFF, BLK, RLOAD)                                   \
  do {                                                                                                   \
    kf = lds_frag(kp + (KOFF)); RLOAD; SBAR();                                                           \
    EX(C0, 0); EX(C0, 1); PIN(C0); SBAR();                                                               \
    PV(o1, VP1, p_hi(N1)); SBAR();                                                                       \
    VN0 = lds_frag(vp + (VOFF)); VP1 = lds_frag(vp + (VOFF) + 608); SBAR();                              \
    EX(C0, 2); EX(C0, 3); PIN(C0); SBAR();                                                               \
    const f32x32 s_ = QKREM(kr, qr, BLK); SBAR();                                                        \
    EX(C0, 4); EX(C0, 5); EX(C0, 6); EX(C0, 7); EX(C0, 8); EX(C0, 9); PIN(C0); SBAR();                   \
    PV(o0, V0, p_lo(C0)); SBAR();                                                                        \
    EX(C0, 10); EX(C0, 11); EX(C0, 12); EX(C0, 13); EX(C0, 14); EX(C0, 15); PIN(C0);                     \
    EX(C1, 0); EX(C1, 1); PIN(C1); SBAR();                                                               \
    PV(o0, V1, p_hi(C0)); SBAR();                                                                        \
    EX(C1, 2); EX(C1, 3); EX(C1, 4); EX(C1, 5); PIN(C1); SBAR();                                         \
    N0 = MFMA32(kf, q0, lo16(s_)); SBAR();                                                               \
    EX(C1, 6); EX(C1, 7); EX(C1, 8); EX(C1, 9); PIN(C1); SBAR();                                         \
    PV(o1, V0, p_lo(C1)); SBAR();                                                                        \
    EX(C1, 10); EX(C1, 11); PIN(C1); SBAR();                                                             \
    N1 = MFMA32(kf, q1, hi16(s_)); SBAR();                                                               \
    EX(C1, 12); EX(C1, 13); EX(C1, 14); EX(C1, 15); PIN(C1); SBAR();                                     \
  } while (0)
#pragma unroll 1
    for (int t = 0; t < 16; ++t) {
      STEP(a0, a1, b0, b1, va0, va1, vb0, vb1, 1024, 1216, 1, (void)0);
      STEP(b0, b1, a0, a1, vb0, vb1, va0, va1, 2048, 2432, 0, kr = *(rptr)(rp + 512));
      kp += 2048; vp += 2432; rp += 512;
    }
    PV(o1, vb1, p_hi(b1));
  }

  auto combine = [&](f32x16& mine) -> float {
    BARRIER();
    __attribute__((address_space(3))) float* xch = (__attribute__((address_space(3))) float*)((lds_ptr)smem + A_END);
    (void)xch;
    __attribute__((address_space(3))) float* buf = (__attribute__((address_space(3))) float*)((lds_ptr)smem + A_K);
    const f32x16 give = kh ? o0 : o1;
    mine = kh ? o1 : o0;
#pragma unroll
    for (int i = 0; i < 13; ++i) buf[(w * 13 + i) * 64 + lane] = give[i];
    BARRIER();
#pragma unroll
    for (int i = 0; i < 13; ++i) mine[i] += buf[((w ^ 4) * 13 + i) * 64 + lane];
    return h ? mine[12] : mine[10];
  };
  f32x16 om;
  float l = combine(om);
  bool ok = l >= 0x1p-64f && l <= 0x1p64f;
#pragma unroll
  for (int i = 0; i < 8; ++i) ok = ok && __builtin_fabsf(om[i]) < __builtin_inff();
  ok = ok && (h || (__builtin_fabsf(om[8]) < __builtin_inff() && __builtin_fabsf(om[9]) < __builtin_inff()));
  if (__syncthreads_or(!ok)) {
#pragma unroll 1
    for (int i = w; i < 64; i += 8) dma16(ws + WS_KM + head * 65536 + i * 1024, lane * 16, lds0 + A_K + i * 1024);
    WAIT_VM(0);
    BARRIER();
    kp = (lds_cptr)smem + A_K + kh * 32768 + lane * 16;
    vp = (lds_cptr)smem + A_V + kh * 38912 + (h * 19 + vrow) * 16;
    rp = (lds_cptr)smem + A_R + kh * 8192 + r * 8;
    float m0 = -__builtin_inff(), m1 = -__builtin_inff();
#pragma unroll 1
    for (int t = 0; t < 32; ++t) {
      const bf16x8 kf = lds_frag(kp + t * 1024);
      const f32x32 s = QKREM(*(rptr)(rp + t * 256), qr, 0);
      const f32x16 s0 = MFMA32(kf, q0, lo16(s)), s1 = MFMA32(kf, q1, hi16(s));
#pragma unroll
      for (int i = 0; i < 16; ++i) { m0 = fmaxf(m0, s0[i]); m1 = fmaxf(m1, s1[i]); }
    }
    m0 = fmaxf(m0, __shfl_xor(m0, 32));
    m1 = fmaxf(m1, __shfl_xor(m1, 32));
    __attribute__((address_space(3))) float* xch = (__attribute__((address_space(3))) float*)((lds_ptr)smem + A_END);
    xch[w * 64 + lane] = h ? m1 : m0;
    BARRIER();
    m0 = fmaxf(m0, xch[(w ^ 4) * 64 + r]);
    m1 = fmaxf(m1, xch[(w ^ 4) * 64 + 32 + r]);
    o0 = f32x16{}; o1 = f32x16{};
#pragma unroll 1
    for (int t = 0; t < 32; ++t) {
      const bf16x8 kf = lds_frag(kp + t * 1024);
      const f32x32 s = QKREM(*(rptr)(rp + t * 256), qr, 0);
      f32x16 s0 = MFMA32(kf, q0, lo16(s)), s1 = MFMA32(kf, q1, hi16(s));
#pragma unroll
      for (int i = 0; i < 16; ++i) { s0[i] = __builtin_amdgcn_exp2f(s0[i] - m0); s1[i] = __builtin_amdgcn_exp2f(s1[i] - m1); }
      const bf16x8 v0 = lds_frag(vp + t * 1216), v1 = lds_frag(vp + t * 1216 + 608);
      PV(o0, v0, p_lo(s0)); PV(o0, v1, p_hi(s0));
      PV(o1, v0, p_lo(s1)); PV(o1, v1, p_hi(s1));
    }
    l = combine(om);
  }
  const float inv = 1.0f / l;
  char* od = ws + WS_O + (size_t)(qt + kh) * 36864;
  *(u32x4*)(od + head * 1024 + lane * 16) =
      u32x4{pack_bf16(om[0] * inv, om[1] * inv), pack_bf16(om[2] * inv, om[3] * inv),
            pack_bf16(om[4] * inv, om[5] * inv), pack_bf16(om[6] * inv, om[7] * inv)};
  if (h == 0) *(unsigned*)(od + 32768 + head * 128 + r * 4) = pack_bf16(om[8] * inv, om[9] * inv);
}

constexpr int O_BUF = 20480, O_AB = 8192, O_NB = 7;

__global__ __launch_bounds__(512, 2) void k_out(const u16* __restrict__ xb, const u16* __restrict__ cre,
                                                const u16* __restrict__ cim, const u16* __restrict__ dskip,
                                                const u16* __restrict__ wo, const char* __restrict__ ws,
                                                float* __restrict__ out) {
  extern __shared__ __attribute__((aligned(16))) char smem[];
  const int xcd = blockIdx.x & 7, slot = blockIdx.x >> 3;
  const int rt = (xcd >> 1) * 8 + (slot & 7), cb = (xcd & 1) * 3 + (slot >> 3);
  const int tid = threadIdx.x, lane = tid & 63;
  const int w = __builtin_amdgcn_readfirstlane(tid >> 6);
  const unsigned lds0 = (unsigned)(size_t)(lds_ptr)smem;
  if (w >= 6) {
    const int lw = w - 6;
    unsigned ho[4], co[6], w0[6], w1[6];
#pragma unroll
    for (int i = 0; i < 4; ++i) {
      const int k = 8 * (4 * lw + i) + (lane >> 3);
      ho[i] = k * 4096 + (((lane & 7) ^ (((k >> 1) & 1) << 2)) << 4);
    }
#pragma unroll
    for (int i = 0; i < 6; ++i) {
      const int row = 8 * (6 * lw + i) + (lane >> 3);
      co[i] = row * 1152 + (((lane & 7) ^ ((row >> 1) & 7)) << 4);
      const int L = (6 * lw + i) * 64 + lane, k = L / 12, c = L % 12;
      const int kk = k & 15, d = (kk & 3) + 8 * ((kk >> 2) & 1) + 4 * (kk >> 3);
      w0[i] = (18 * (k >> 4) + d) * 1152 + c * 16;
      w1[i] = (18 * (k >> 1) + 16 + (k & 1)) * 1152 + c * 16;
    }
    const char* hs = ws + WS_H + rt * 128;
    const char* os = ws + WS_O + (size_t)(2 * rt + lw) * 36864;
    const char* wos = (const char*)wo + cb * 192;
    int slotb = 0;
    auto issue = [&](int c) {
      const unsigned bb = lds0 + slotb * O_BUF;
      slotb = slotb == O_NB - 1 ? 0 : slotb + 1;
      if (c < 18) {
        const char* cs = (const char*)(c < 9 ? cre : cim) + cb * 96 * 1152 + (c < 9 ? c : c - 9) * 128;
#pragma unroll
        for (int i = 0; i < 4; ++i) dma16(hs + (size_t)c * 64 * 4096, ho[i], bb + (4 * lw + i) * 1024);
#pragma unroll
        for (int i = 0; i < 6; ++i) dma16(cs, co[i], bb + O_AB + (6 * lw + i) * 1024);
      } else {
        const int e = c - 18;
#pragma unroll
        for (int i = 0; i < 4; ++i) dma16(os + e * 4096 + i * 1024, lane * 16, bb + (4 * lw + i) * 1024);
#pragma unroll
        for (int i = 0; i < 6; ++i) dma16(wos + (e < 8 ? e * 72 * 1152 : 0), e < 8 ? w0[i] : w1[i], bb + O_AB + (6 * lw + i) * 1024);
      }
    };
#pragma unroll 1
    for (int c = 0; c < 6; ++c) issue(c);
#pragma unroll 1
    for (int c = 0; c < 21; ++c) { WAIT_VM(50); BARRIER(); issue(c + 6); }
    WAIT_VM(50); BARRIER();
    WAIT_VM(40); BARRIER();
    WAIT_VM(30); BARRIER();
    WAIT_VM(20); BARRIER();
    WAIT_VM(10); BARRIER();
    WAIT_VM(0); BARRIER();
    return;
  }
  const int r = lane & 31, h = lane >> 5, ti = w & 1, tj = w >> 1;
  const int blk = (lane >> 4) & 1, q = (lane & 15) >> 2, p = lane & 3;
  lds_cptr lds = (lds_cptr)smem;
  const int tok0 = rt * 64 + ti * 32, col = cb * 96 + tj * 32 + r;
  u16 xv[16];
#pragma unroll
  for (int i = 0; i < 16; ++i) xv[i] = xb[(tok0 + crow(i, h)) * DM + col];
  const float dsk = bf_val(dskip[col]);
  int boff = 0;
  {
    const int ha = (8 * h + q) * 128 + (((4 * ti + 2 * blk + (p >> 1)) ^ ((q >> 1) << 2)) << 4) + 8 * (p & 1);
    int ca[4];
#pragma unroll
    for (int ks = 0; ks < 4; ++ks) ca[ks] = O_AB + (32 * tj + r) * 128 + (((2 * ks + h) ^ ((r >> 1) & 7)) << 4);
    f32x16 acc = {};
#pragma unroll 1
    for (int c = 0; c < 18; ++c) {
      BARRIER();
      bf16x8 af[4], bf[4];
#pragma unroll
      for (int ks = 0; ks < 4; ++ks) { af[ks] = lds_frag_tr(lds + boff + ha + ks * 2048, 512); bf[ks] = lds_frag(lds + boff + ca[ks]); }
#pragma unroll
      for (int ks = 0; ks < 4; ++ks) acc = MFMA32(af[ks], bf[ks], acc);
      boff = boff == (O_NB - 1) * O_BUF ? 0 : boff + O_BUF;
    }
#pragma unroll
    for (int i = 0; i < 16; ++i) {
      const float skip = bf_val(__builtin_bit_cast(u16, (__bf16)(bf_val(xv[i]) * dsk)));
      out[(size_t)(tok0 + crow(i, h)) * 1152 + 576 + col] = acc[i] + skip;
    }
  }
  {
    const int wa = O_AB + (8 * h + q) * 192 + (4 * tj + 2 * blk + (p >> 1)) * 16 + 8 * (p & 1);
    f32x16 acc = {};
#pragma unroll 1
    for (int c = 0; c < 8; ++c) {
      BARRIER();
      bf16x8 af[4], bf[4];
#pragma unroll
      for (int ks = 0; ks < 4; ++ks) { af[ks] = lds_frag(lds + boff + ti * 4096 + ks * 1024 + lane * 16); bf[ks] = lds_frag_tr(lds + boff + wa + ks * 3072, 768); }
#pragma unroll
      for (int ks = 0; ks < 4; ++ks) acc = MFMA32(af[ks], bf[ks], acc);
      boff = boff == (O_NB - 1) * O_BUF ? 0 : boff + O_BUF;
    }
    BARRIER();
#pragma unroll
    for (int ks = 0; ks < 4; ++ks) {
      typedef const __attribute__((address_space(3))) unsigned* up;
      const up s = (up)(lds + boff + ti * 4096 + (8 * ks + 4 * h) * 128 + r * 4);
      const u32x4 a = {s[0], s[32], s[64], s[96]};
      acc = MFMA32(__builtin_bit_cast(bf16x8, a), lds_frag_tr(lds + boff + wa + ks * 3072, 768), acc);
    }
#pragma unroll
    for (int i = 0; i < 16; ++i) out[(size_t)(tok0 + crow(i, h)) * 1152 + col] = acc[i];
  }
}

extern "C" void kernel_launch(void* const* d_in, const int* in_sizes, int n_in, void* d_out, int out_size,
                              void* d_ws, size_t ws_size, hipStream_t stream) {
  const u16* xa = (const u16*)d_in[0];
  const u16* wqkv = (const u16*)d_in[1];
  const u16* wo = (const u16*)d_in[2];
  const u16* xb = (const u16*)d_in[3];
  const u16* lre = (const u16*)d_in[4];
  const u16* lim = (const u16*)d_in[5];
  const u16* bre = (const u16*)d_in[6];
  const u16* bim = (const u16*)d_in[7];
  const u16* cre = (const u16*)d_in[8];
  const u16* cim = (const u16*)d_in[9];
  const u16* logdt = (const u16*)d_in[10];
  const u16* dskip = (const u16*)d_in[11];
  char* ws = (char*)d_ws;
  constexpr int L1 = 4 * P_BUF, L2 = A_END + 2048 + 1024, L3 = O_NB * O_BUF;
  (void)hipFuncSetAttribute((const void*)k_proj, hipFuncAttributeMaxDynamicSharedMemorySize, L1);
  (void)hipFuncSetAttribute((const void*)k_attn, hipFuncAttributeMaxDynamicSharedMemorySize, L2);
  (void)hipFuncSetAttribute((const void*)k_out, hipFuncAttributeMaxDynamicSharedMemorySize, L3);
  k_proj<<<256, 512, L1, stream>>>(xa, wqkv, xb, bre, bim, lre, lim, logdt, ws);
  k_attn<<<256, 512, L2, stream>>>(ws);
  k_out<<<192, 512, L3, stream>>>(xb, cre, cim, dskip, wo, ws, (float*)d_out);
}
```

```cpp
#define DIAG
#include <cstdio>
#include <hip/hip_runtime.h>
#include <hip/hip_bf16.h>

typedef unsigned short u16;
typedef __attribute__((ext_vector_type(8))) __bf16 bf16x8;
typedef __attribute__((ext_vector_type(16))) __bf16 bf16x16;
typedef __attribute__((ext_vector_type(2))) __bf16 bf16x2;
typedef __attribute__((ext_vector_type(4))) short s16x4;
typedef __attribute__((ext_vector_type(2))) float f32x2;
typedef __attribute__((ext_vector_type(8))) float f32x8;
typedef __attribute__((ext_vector_type(16))) float f32x16;
typedef __attribute__((ext_vector_type(32))) float f32x32;
typedef __attribute__((ext_vector_type(2))) unsigned u32x2;
typedef __attribute__((ext_vector_type(4))) unsigned u32x4;
typedef __attribute__((address_space(3))) const char* lds_cptr;
typedef __attribute__((address_space(3))) char* lds_ptr;

#define DI __device__ __forceinline__
#define SBAR() __builtin_amdgcn_sched_barrier(0)
#define PIN(x) asm volatile("" : "+v"(x))

constexpr int SEQ = 2048, DM = 576, NH = 32;

constexpr size_t WS_QM = 0;
constexpr size_t WS_QR = WS_QM + 32 * 65536;
constexpr size_t WS_KM = WS_QR + 16 * 16384;
constexpr size_t WS_KR = WS_KM + 32 * 65536;
constexpr size_t WS_V = WS_KR + 16 * 16384;
constexpr size_t WS_U = WS_V + 32 * 77824;
constexpr size_t WS_H = WS_U + (size_t)576 * 8192;
constexpr size_t WS_O = WS_H + (size_t)1152 * 4096;
constexpr size_t WS_TAB = WS_O + (size_t)64 * 36864;

DI unsigned pack_bf16(float lo, float hi) {
  f32x2 v = {lo, hi};
  return __builtin_bit_cast(unsigned, __builtin_convertvector(v, bf16x2));
}
DI float bf_lo(unsigned x) { return __uint_as_float(x << 16); }
DI float bf_hi(unsigned x) { return __uint_as_float(x & 0xffff0000u); }
DI float bf_val(u16 x) { return __uint_as_float((unsigned)x << 16); }

DI void dma16(const void* base, unsigned voff, unsigned lds_dst) {
  asm volatile("s_mov_b32 m0, %2\n\ts_nop 0\n\tglobal_load_lds_dwordx4 %0, %1" ::"v"(voff), "s"(base), "s"(lds_dst) : "memory");
}
#define WAIT_VM(n) asm volatile("s_waitcnt vmcnt(" #n ")" ::: "memory")
#define BARRIER() asm volatile("s_waitcnt lgkmcnt(0)\n\ts_barrier" ::: "memory")

DI bf16x8 lds_frag(lds_cptr p) { return *(const __attribute__((address_space(3))) bf16x8*)p; }
DI bf16x8 lds_frag_tr(lds_cptr p, int second) {
  typedef __attribute__((address_space(3))) s16x4* tp;
  const s16x4 lo = __builtin_amdgcn_ds_read_tr16_b64_v4i16((tp)p);
  const s16x4 hi = __builtin_amdgcn_ds_read_tr16_b64_v4i16((tp)(p + second));
  return __builtin_bit_cast(bf16x8, __builtin_shufflevector(lo, hi, 0, 1, 2, 3, 4, 5, 6, 7));
}
#define MFMA32(a, b, c) __builtin_amdgcn_mfma_f32_32x32x16_bf16((a), (b), (c), 0, 0, 0)

constexpr size_t WS_DIAG = (size_t)64 << 20;
DI void stamp(const char* ws, int kern, int w, int idx) {
  unsigned long long t, rt;
  SBAR();
  asm volatile("s_memtime %0\n\ts_memrealtime %1\n\ts_waitcnt lgkmcnt(0)" : "=s"(t), "=s"(rt)::"memory");
  SBAR();
  if ((threadIdx.x & 63) == 0) {
    unsigned long long* p = (unsigned long long*)(ws + WS_DIAG) + ((((size_t)kern * 256 + blockIdx.x) * 8 + w) * 16 + idx) * 2;
    p[0] = t; p[1] = rt;
  }
}
#define STAMP(k, i) stamp(ws, k, w, i)
#define DIAG_NS0 5
#define DIAG_NS1 6
#define DIAG_NS2 5
DI int crow(int reg, int h) { return (reg & 3) + 8 * (reg >> 2) + 4 * h; }

constexpr int P_BUF = 40960, P_XB = 16384;

template <int MODE>
DI void proj_compute(lds_cptr lds, int w, int lane, f32x16 (&acc)[2][3], const char* ws) {
  const int r = lane & 31, h = lane >> 5, tw = w & 1, cw = w >> 1;
  int xa[4], wa[4];
#pragma unroll
  for (int ks = 0; ks < 4; ++ks) xa[ks] = (64 * tw + r) * 128 + (((2 * ks + h) ^ ((r >> 1) & 7)) << 4);
  if (MODE == 2) {
    const int row = (r & 1) * 96 + 48 * cw + (r >> 1);
    const int f = ((row >> 1) & 7) ^ (r & 1);
#pragma unroll
    for (int ks = 0; ks < 4; ++ks) wa[ks] = P_XB + row * 128 + (((2 * ks + h) ^ f) << 4);
  } else {
    const int blk = (lane >> 4) & 1, q = (lane & 15) >> 2, p = lane & 3;
#pragma unroll
    for (int j = 0; j < 3; ++j)
      wa[j] = P_XB + (8 * h + q) * 384 + (((12 * cw + 4 * j + 2 * blk + (p >> 1)) ^ ((q >> 1) << 2)) << 4) + 8 * (p & 1);
    wa[3] = 0;
  }
  bf16x8 xf[2][2], wf[2][3];
  auto load = [&](int s, int boff, int ks) {
#pragma unroll
    for (int i = 0; i < 2; ++i) xf[s][i] = lds_frag(lds + boff + xa[ks] + i * 4096);
#pragma unroll
    for (int j = 0; j < 3; ++j) {
      if (MODE == 2) wf[s][j] = lds_frag(lds + boff + wa[ks] + j * 2048);
      else wf[s][j] = lds_frag_tr(lds + boff + wa[j] + ks * 6144, 1536);
    }
  };
  auto mma = [&](int s) {
#pragma unroll
    for (int j = 0; j < 3; ++j)
#pragma unroll
      for (int i = 0; i < 2; ++i) {
        if (MODE == 1) acc[i][j] = MFMA32(xf[s][i], wf[s][j], acc[i][j]);
        else acc[i][j] = MFMA32(wf[s][j], xf[s][i], acc[i][j]);
      }
  };
  BARRIER();
  STAMP(0, 1);
  load(0, 0, 0);
  int boff = 0;
#pragma unroll 1
  for (int c = 0; c < 9; ++c) {
    load(1, boff, 1); SBAR(); mma(0); SBAR();
    load(0, boff, 2); SBAR(); mma(1); SBAR();
    load(1, boff, 3); SBAR(); mma(0); SBAR();
    boff = (boff == 3 * P_BUF) ? 0 : boff + P_BUF;
    if (c < 8) { BARRIER(); load(0, boff, 0); }
    SBAR(); mma(1); SBAR();
  }
  STAMP(0, 2);
}

DI void ssm_table(const u16* lre, const u16* lim, const u16* logdt, float* tab, int p) {
  const float dt = bf_val(__builtin_bit_cast(u16, (__bf16)expf(bf_val(logdt[p]))));
  const float lr = bf_val(lre[p]), li = bf_val(lim[p]);
  const float mag = expf(dt * lr);
  const float ang = dt * li;
  const float ar = mag * cosf(ang), ai = mag * sinf(ang);
  const double nr = (double)ar - 1.0, ni = ai, den = (double)lr * lr + (double)li * li;
  float* t = tab + p * 16;
  t[0] = ar; t[1] = ai;
  t[2] = (float)((nr * lr + ni * li) / den);
  t[3] = (float)((ni * lr - nr * li) / den);
  double pr = ar, pi = ai;
  for (int i = 0; i < 5; ++i) { const double n = pr * pr - pi * pi; pi = 2.0 * pr * pi; pr = n; }
  for (int i = 0; i < 6; ++i) {
    t[4 + 2 * i] = (float)pr; t[5 + 2 * i] = (float)pi;
    const double n = pr * pr - pi * pi; pi = 2.0 * pr * pi; pr = n;
  }
}

__global__ __launch_bounds__(512, 2) void k_proj(const u16* __restrict__ xa, const u16* __restrict__ wqkv,
                                                 const u16* __restrict__ xb, const u16* __restrict__ bre,
                                                 const u16* __restrict__ bim, const u16* __restrict__ lre,
                                                 const u16* __restrict__ lim, const u16* __restrict__ logdt,
                                                 char* __restrict__ ws) {
  extern __shared__ __attribute__((aligned(16))) char smem[];
  const int xcd = blockIdx.x & 7, slot = blockIdx.x >> 3;
  const int cb = (xcd & 1) * 8 + (slot >> 2), rt = (xcd >> 1) * 4 + (slot & 3);
  const int tid = threadIdx.x, lane = tid & 63;
  const int w = __builtin_amdgcn_readfirstlane(tid >> 6);
  STAMP(0, 0);
  if (cb == 15) {
    if (tid < 36) ssm_table(lre, lim, logdt, (float*)(ws + WS_TAB), rt * 36 + tid);
    return;
  }
  const unsigned lds0 = (unsigned)(size_t)(lds_ptr)smem;
  if (w >= 4) {
    const int lw = w - 4;
    unsigned xo[4], wo[6];
#pragma unroll
    for (int i = 0; i < 4; ++i) {
      const int row = 8 * (4 * lw + i) + (lane >> 3);
      xo[i] = row * 1152 + (((lane & 7) ^ ((row >> 1) & 7)) << 4);
    }
    const char *xs, *wsrc;
    unsigned wstep;
    if (cb < 9) {
      xs = (const char*)xa;
      wsrc = (const char*)wqkv + cb * 384;
      wstep = 64 * 3456;
#pragma unroll
      for (int i = 0; i < 6; ++i) {
        const int L = (6 * lw + i) * 64 + lane, k = L / 24, c = L % 24;
        wo[i] = k * 3456 + ((c ^ (((k >> 1) & 1) << 2)) << 4);
      }
    } else {
      xs = (const char*)xb;
      wsrc = (const char*)(lw < 2 ? bre : bim) + (cb - 9) * 96 * 1152;
      wstep = 128;
#pragma unroll
      for (int i = 0; i < 6; ++i) {
        const int row = 8 * (6 * (lw & 1) + i) + (lane >> 3);
        wo[i] = row * 1152 + (((lane & 7) ^ ((row >> 1) & 7) ^ (lw >> 1)) << 4);
      }
    }
    xs += rt * 128 * 1152;
    auto issue = [&](int c) {
      const unsigned b = lds0 + (c & 3) * P_BUF;
#pragma unroll
      for (int i = 0; i < 4; ++i) dma16(xs + c * 128, xo[i], b + (4 * lw + i) * 1024);
#pragma unroll
      for (int i = 0; i < 6; ++i) dma16(wsrc + (size_t)c * wstep, wo[i], b + P_XB + (6 * lw + i) * 1024);
    };
#pragma unroll 1
    for (int c = 0; c < 4; ++c) issue(c);
    WAIT_VM(30); BARRIER();
#pragma unroll 1
    for (int c = 1; c < 6; ++c) { WAIT_VM(20); BARRIER(); issue(c + 3); }
    WAIT_VM(20); BARRIER();
    WAIT_VM(10); BARRIER();
    WAIT_VM(0); BARRIER();
    return;
  }
  const int r = lane & 31, h = lane >> 5, tw = w & 1, cw = w >> 1;
  f32x16 acc[2][3];
#pragma unroll
  for (int i = 0; i < 2; ++i)
#pragma unroll
    for (int j = 0; j < 3; ++j) acc[i][j] = f32x16{};
  const int tile0 = rt * 4 + tw * 2;
  if (cb < 6) {
    proj_compute<0>((lds_cptr)smem, w, lane, acc, ws);
    const bool isq = cb < 3;
    const float sc = isq ? 0.23570226039551584f * 1.4426950408889634f : 1.0f;
    const int sl = isq ? r : ((r & 19) | ((r & 4) << 1) | ((r & 8) >> 1));
    char* mainb = ws + (isq ? WS_QM : WS_KM);
    constexpr unsigned REM = WS_QR - WS_QM;
#pragma unroll
    for (int j = 0; j < 3; ++j)
#pragma unroll
      for (int m = 0; m < 4; ++m) {
        const int G = (isq ? cb : cb - 3) * 48 + cw * 24 + j * 8 + 2 * m + h;
        const int pair = G / 9, pos = G - 9 * pair;
        const int qd = pos > 4 ? pos - 5 : pos;
        const unsigned mo = (2 * pair + (pos > 4)) * 65536 + tile0 * 1024 + ((qd >> 1) * 32 + sl) * 16 + (qd & 1) * 8;
        const unsigned ro = REM + pair * 16384 + (tile0 * 32 + sl) * 8;
        const unsigned off = pos == 4 ? ro : mo, ts = pos == 4 ? 256 : 1024;
#pragma unroll
        for (int i = 0; i < 2; ++i) {
          const u32x2 v = {pack_bf16(acc[i][j][4 * m] * sc, acc[i][j][4 * m + 1] * sc),
                           pack_bf16(acc[i][j][4 * m + 2] * sc, acc[i][j][4 * m + 3] * sc)};
          *(u32x2*)(mainb + off + i * ts) = v;
        }
      }
  } else if (cb < 9) {
    proj_compute<1>((lds_cptr)smem, w, lane, acc, ws);
#pragma unroll
    for (int j = 0; j < 3; ++j) {
      const int n = (cb - 6) * 192 + cw * 96 + j * 32 + r, head = n / 18, d = n - 18 * head;
      char* dst = ws + WS_V + head * 77824 + tile0 * 1216 + (h * 19 + d) * 16;
#pragma unroll
      for (int i = 0; i < 2; ++i)
#pragma unroll
        for (int s = 0; s < 2; ++s) {
          const f32x16& a = acc[i][j];
          const u32x4 v = {pack_bf16(a[8 * s], a[8 * s + 1]), pack_bf16(a[8 * s + 2], a[8 * s + 3]),
                           pack_bf16(a[8 * s + 4], a[8 * s + 5]), pack_bf16(a[8 * s + 6], a[8 * s + 7])};
          *(u32x4*)(dst + i * 1216 + s * 608) = v;
        }
      if (d == 17) {
        const u32x4 ones = {0x3f803f80u, 0x3f803f80u, 0x3f803f80u, 0x3f803f80u};
#pragma unroll
        for (int i = 0; i < 4; ++i) *(u32x4*)(dst + 16 + i * 608) = ones;
      }
    }
  } else {
    proj_compute<2>((lds_cptr)smem, w, lane, acc, ws);
    char* dst = ws + WS_U + ((cb - 9) * 96 + cw * 48 + 2 * h) * 8192 + (tile0 * 32 + r) * 4;
#pragma unroll
    for (int j = 0; j < 3; ++j)
#pragma unroll
      for (int t = 0; t < 8; ++t)
#pragma unroll
        for (int i = 0; i < 2; ++i)
          *(unsigned*)(dst + (16 * j + 4 * (t >> 1) + (t & 1)) * 8192 + i * 128) = pack_bf16(acc[i][j][2 * t], acc[i][j][2 * t + 1]);
  }
  STAMP(0, 3);
  WAIT_VM(0);
  STAMP(0, 4);
}

constexpr int A_K = 0, A_V = 65536, A_R = A_V + 77824, A_END = A_R + 16384;

DI f32x2 cfma(f32x2 a, f32x2 x, f32x2 c) {
  return f32x2{__builtin_fmaf(a[0], x[0], __builtin_fmaf(-a[1], x[1], c[0])),
               __builtin_fmaf(a[0], x[1], __builtin_fmaf(a[1], x[0], c[1]))};
}

DI void ssm_scan(char* ws, int p, int lane, const u32x4 (&u)[8]) {
  const float* t = (const float*)(ws + WS_TAB) + p * 16;
  const f32x2 a = {t[0], t[1]};
  f32x2 e = {0.f, 0.f};
#pragma unroll
  for (int i = 0; i < 32; ++i) e = cfma(a, e, f32x2{bf_lo(u[i >> 2][i & 3]), bf_hi(u[i >> 2][i & 3])});
#pragma unroll
  for (int s = 0; s < 6; ++s) {
    const int d = 1 << s;
    f32x2 o = {__shfl_up(e[0], d), __shfl_up(e[1], d)};
    if (lane < d) o = f32x2{0.f, 0.f};
    e = cfma(f32x2{t[4 + 2 * s], t[5 + 2 * s]}, o, e);
  }
  f32x2 hs = {__shfl_up(e[0], 1), __shfl_up(e[1], 1)};
  if (lane == 0) hs = f32x2{0.f, 0.f};
  const float cr = 2.f * t[2], ci = 2.f * t[3];
  char* dre = ws + WS_H + (size_t)p * 4096 + lane * 64;
  char* dim = dre + (size_t)576 * 4096;
#pragma unroll
  for (int g = 0; g < 4; ++g) {
    float re[8], im[8];
#pragma unroll
    for (int i = 0; i < 8; ++i) {
      const unsigned x = u[2 * g + (i >> 2)][i & 3];
      hs = cfma(a, hs, f32x2{bf_lo(x), bf_hi(x)});
      re[i] = cr * hs[0] - ci * hs[1];
      im[i] = -cr * hs[1] - ci * hs[0];
    }
    *(u32x4*)(dre + g * 16) = u32x4{pack_bf16(re[0], re[1]), pack_bf16(re[2], re[3]), pack_bf16(re[4], re[5]), pack_bf16(re[6], re[7])};
    *(u32x4*)(dim + g * 16) = u32x4{pack_bf16(im[0], im[1]), pack_bf16(im[2], im[3]), pack_bf16(im[4], im[5]), pack_bf16(im[6], im[7])};
  }
}

DI f32x16 lo16(const f32x32& x) { return __builtin_shufflevector(x, x, 0, 1, 2, 3, 4, 5, 6, 7, 8, 9, 10, 11, 12, 13, 14, 15); }
DI f32x16 hi16(const f32x32& x) { return __builtin_shufflevector(x, x, 16, 17, 18, 19, 20, 21, 22, 23, 24, 25, 26, 27, 28, 29, 30, 31); }
DI bf16x16 p_lo(const f32x16& x) { return __builtin_bit_cast(bf16x16, __builtin_shufflevector(x, x, 0, 1, 2, 3, 4, 5, 6, 7)); }
DI bf16x16 p_hi(const f32x16& x) { return __builtin_bit_cast(bf16x16, __builtin_shufflevector(x, x, 8, 9, 10, 11, 12, 13, 14, 15)); }
#define PV(o, v, p) o = __builtin_amdgcn_smfmac_f32_32x32x32_bf16((v), (p), (o), sidx, 0, 0)
#define QKREM(kr, qr, blk) __builtin_amdgcn_mfma_f32_32x32x4bf16_1k((kr), (qr), f32x32{}, 1, (blk), 0)
#define EX(x, i) x[i] = __builtin_amdgcn_exp2f(x[i])

__global__ __launch_bounds__(512, 2) void k_attn(char* __restrict__ ws) {
  extern __shared__ __attribute__((aligned(16))) char smem[];
  const int b = blockIdx.x;
  const int head = 4 * (b & 7) + ((b >> 3) & 3), qb = b >> 5;
  const int tid = threadIdx.x, lane = tid & 63;
  const int w = __builtin_amdgcn_readfirstlane(tid >> 6);
  const int r = lane & 31, h = lane >> 5, qw = w & 3, kh = w >> 2;
  const unsigned lds0 = (unsigned)(size_t)(lds_ptr)smem;

  STAMP(1, 0);
  const bool scan = b < 192 && w < 3;
  const int chan = 3 * b + w;
  u32x4 u[8];
  if (scan) {
#pragma unroll
    for (int i = 0; i < 8; ++i) u[i] = *(const u32x4*)(ws + WS_U + (size_t)chan * 8192 + lane * 128 + i * 16);
  }
  const int qt = qb * 8 + qw * 2;
  const bf16x8 q0 = *(const bf16x8*)(ws + WS_QM + head * 65536 + qt * 1024 + lane * 16);
  const bf16x8 q1 = *(const bf16x8*)(ws + WS_QM + head * 65536 + qt * 1024 + 1024 + lane * 16);
  const unsigned qrw = *(const unsigned*)(ws + WS_QR + (head >> 1) * 16384 + (qt * 32 + lane) * 8 + (head & 1) * 4);
  {
    const unsigned vo = lane * 16;
#pragma unroll 1
    for (int i = w; i < 64; i += 8) dma16(ws + WS_KM + head * 65536 + i * 1024, vo, lds0 + A_K + i * 1024);
#pragma unroll 1
    for (int i = w; i < 76; i += 8) dma16(ws + WS_V + head * 77824 + i * 1024, vo, lds0 + A_V + i * 1024);
#pragma unroll 1
    for (int i = w; i < 16; i += 8) dma16(ws + WS_KR + (head >> 1) * 16384 + i * 1024, vo, lds0 + A_R + i * 1024);
  }
  STAMP(1, 1);
  if (scan) ssm_scan(ws, chan, lane, u);
  STAMP(1, 2);
  const u32x2 qru = (head & 1) ? u32x2{0u, qrw} : u32x2{qrw, 0u};
  const s16x4 qr = __builtin_bit_cast(s16x4, qru);
  WAIT_VM(0);
  BARRIER();
  STAMP(1, 3);

  const int vrow = r < 19 ? r : (r >= 20 && r < 28 ? 0 : 18);
  lds_cptr kp = (lds_cptr)smem + A_K + kh * 32768 + lane * 16;
  lds_cptr vp = (lds_cptr)smem + A_V + kh * 38912 + (h * 19 + vrow) * 16;
  lds_cptr rp = (lds_cptr)smem + A_R + kh * 8192 + lane * 8;
  typedef const __attribute__((address_space(3))) s16x4* rptr;
  int sidx = 0xDDDD;
  PIN(sidx);
  f32x16 o0 = {}, o1 = {};
  {
    f32x16 a0, a1, b0, b1;
    bf16x8 kf, va0, va1, vb0, vb1;
    s16x4 kr = *(rptr)rp;
    kf = lds_frag(kp);
    va0 = lds_frag(vp); va1 = lds_frag(vp + 608);
    { const f32x32 s = QKREM(kr, qr, 0); a0 = MFMA32(kf, q0, lo16(s)); a1 = MFMA32(kf, q1, hi16(s)); }
    vb1 = va1;
    b1 = f32x16{};
#define STEP(C0, C1, N0, N1, V0, V1, VN0, VP1, KOFF, VOFF, BLK, RLOAD)                                   \
  do {                                                                                                   \
    kf = lds_frag(kp + (KOFF)); RLOAD; SBAR();                                                           \
    EX(C0, 0); EX(C0, 1); PIN(C0); SBAR();                                                               \
    PV(o1, VP1, p_hi(N1)); SBAR();                                                                       \
    VN0 = lds_frag(vp + (VOFF)); VP1 = lds_frag(vp + (VOFF) + 608); SBAR();                              \
    EX(C0, 2); EX(C0, 3); PIN(C0); SBAR();                                                               \
    const f32x32 s_ = QKREM(kr, qr, BLK); SBAR();                                                        \
    EX(C0, 4); EX(C0, 5); EX(C0, 6); EX(C0, 7); EX(C0, 8); EX(C0, 9); PIN(C0); SBAR();                   \
    PV(o0, V0, p_lo(C0)); SBAR();                                                                        \
    EX(C0, 10); EX(C0, 11); EX(C0, 12); EX(C0, 13); EX(C0, 14); EX(C0, 15); PIN(C0);                     \
    EX(C1, 0); EX(C1, 1); PIN(C1); SBAR();                                                               \
    PV(o0, V1, p_hi(C0)); SBAR();                                                                        \
    EX(C1, 2); EX(C1, 3); EX(C1, 4); EX(C1, 5); PIN(C1); SBAR();                                         \
    N0 = MFMA32(kf, q0, lo16(s_)); SBAR();                                                               \
    EX(C1, 6); EX(C1, 7); EX(C1, 8); EX(C1, 9); PIN(C1); SBAR();                                         \
    PV(o1, V0, p_lo(C1)); SBAR();                                                                        \
    EX(C1, 10); EX(C1, 11); PIN(C1); SBAR();                                                             \
    N1 = MFMA32(kf, q1, hi16(s_)); SBAR();                                                               \
    EX(C1, 12); EX(C1, 13); EX(C1, 14); EX(C1, 15); PIN(C1); SBAR();                                     \
  } while (0)
#pragma unroll 1
    for (int t = 0; t < 16; ++t) {
      STEP(a0, a1, b0, b1, va0, va1, vb0, vb1, 1024, 1216, 1, (void)0);
      STEP(b0, b1, a0, a1, vb0, vb1, va0, va1, 2048, 2432, 0, kr = *(rptr)(rp + 512));
      kp += 2048; vp += 2432; rp += 512;
    }
    PV(o1, vb1, p_hi(b1));
  }
  STAMP(1, 4);

  auto combine = [&](f32x16& mine) -> float {
    BARRIER();
    __attribute__((address_space(3))) float* xch = (__attribute__((address_space(3))) float*)((lds_ptr)smem + A_END);
    (void)xch;
    __attribute__((address_space(3))) float* buf = (__attribute__((address_space(3))) float*)((lds_ptr)smem + A_K);
    const f32x16 give = kh ? o0 : o1;
    mine = kh ? o1 : o0;
#pragma unroll
    for (int i = 0; i < 13; ++i) buf[(w * 13 + i) * 64 + lane] = give[i];
    BARRIER();
#pragma unroll
    for (int i = 0; i < 13; ++i) mine[i] += buf[((w ^ 4) * 13 + i) * 64 + lane];
    return h ? mine[12] : mine[10];
  };
  f32x16 om;
  float l = combine(om);
  bool ok = l >= 0x1p-64f && l <= 0x1p64f;
#pragma unroll
  for (int i = 0; i < 8; ++i) ok = ok && __builtin_fabsf(om[i]) < __builtin_inff();
  ok = ok && (h || (__builtin_fabsf(om[8]) < __builtin_inff() && __builtin_fabsf(om[9]) < __builtin_inff()));
  if (__syncthreads_or(!ok)) {
#pragma unroll 1
    for (int i = w; i < 64; i += 8) dma16(ws + WS_KM + head * 65536 + i * 1024, lane * 16, lds0 + A_K + i * 1024);
    WAIT_VM(0);
    BARRIER();
    kp = (lds_cptr)smem + A_K + kh * 32768 + lane * 16;
    vp = (lds_cptr)smem + A_V + kh * 38912 + (h * 19 + vrow) * 16;
    rp = (lds_cptr)smem + A_R + kh * 8192 + r * 8;
    float m0 = -__builtin_inff(), m1 = -__builtin_inff();
#pragma unroll 1
    for (int t = 0; t < 32; ++t) {
      const bf16x8 kf = lds_frag(kp + t * 1024);
      const f32x32 s = QKREM(*(rptr)(rp + t * 256), qr, 0);
      const f32x16 s0 = MFMA32(kf, q0, lo16(s)), s1 = MFMA32(kf, q1, hi16(s));
#pragma unroll
      for (int i = 0; i < 16; ++i) { m0 = fmaxf(m0, s0[i]); m1 = fmaxf(m1, s1[i]); }
    }
    m0 = fmaxf(m0, __shfl_xor(m0, 32));
    m1 = fmaxf(m1, __shfl_xor(m1, 32));
    __attribute__((address_space(3))) float* xch = (__attribute__((address_space(3))) float*)((lds_ptr)smem + A_END);
    xch[w * 64 + lane] = h ? m1 : m0;
    BARRIER();
    m0 = fmaxf(m0, xch[(w ^ 4) * 64 + r]);
    m1 = fmaxf(m1, xch[(w ^ 4) * 64 + 32 + r]);
    o0 = f32x16{}; o1 = f32x16{};
#pragma unroll 1
    for (int t = 0; t < 32; ++t) {
      const bf16x8 kf = lds_frag(kp + t * 1024);
      const f32x32 s = QKREM(*(rptr)(rp + t * 256), qr, 0);
      f32x16 s0 = MFMA32(kf, q0, lo16(s)), s1 = MFMA32(kf, q1, hi16(s));
#pragma unroll
      for (int i = 0; i < 16; ++i) { s0[i] = __builtin_amdgcn_exp2f(s0[i] - m0); s1[i] = __builtin_amdgcn_exp2f(s1[i] - m1); }
      const bf16x8 v0 = lds_frag(vp + t * 1216), v1 = lds_frag(vp + t * 1216 + 608);
      PV(o0, v0, p_lo(s0)); PV(o0, v1, p_hi(s0));
      PV(o1, v0, p_lo(s1)); PV(o1, v1, p_hi(s1));
    }
    l = combine(om);
  }
  const float inv = 1.0f / l;
  char* od = ws + WS_O + (size_t)(qt + kh) * 36864;
  *(u32x4*)(od + head * 1024 + lane * 16) =
      u32x4{pack_bf16(om[0] * inv, om[1] * inv), pack_bf16(om[2] * inv, om[3] * inv),
            pack_bf16(om[4] * inv, om[5] * inv), pack_bf16(om[6] * inv, om[7] * inv)};
  if (h == 0) *(unsigned*)(od + 32768 + head * 128 + r * 4) = pack_bf16(om[8] * inv, om[9] * inv);
  STAMP(1, 5);
}

constexpr int O_BUF = 20480, O_AB = 8192, O_NB = 7;

__global__ __launch_bounds__(512, 2) void k_out(const u16* __restrict__ xb, const u16* __restrict__ cre,
                                                const u16* __restrict__ cim, const u16* __restrict__ dskip,
                                                const u16* __restrict__ wo, const char* __restrict__ ws,
                                                float* __restrict__ out) {
  extern __shared__ __attribute__((aligned(16))) char smem[];
  const int xcd = blockIdx.x & 7, slot = blockIdx.x >> 3;
  const int rt = (xcd >> 1) * 8 + (slot & 7), cb = (xcd & 1) * 3 + (slot >> 3);
  const int tid = threadIdx.x, lane = tid & 63;
  const int w = __builtin_amdgcn_readfirstlane(tid >> 6);
  const unsigned lds0 = (unsigned)(size_t)(lds_ptr)smem;
  STAMP(2, 0);
  if (w >= 6) {
    const int lw = w - 6;
    unsigned ho[4], co[6], w0[6], w1[6];
#pragma unroll
    for (int i = 0; i < 4; ++i) {
      const int k = 8 * (4 * lw + i) + (lane >> 3);
      ho[i] = k * 4096 + (((lane & 7) ^ (((k >> 1) & 1) << 2)) << 4);
    }
#pragma unroll
    for (int i = 0; i < 6; ++i) {
      const int row = 8 * (6 * lw + i) + (lane >> 3);
      co[i] = row * 1152 + (((lane & 7) ^ ((row >> 1) & 7)) << 4);
      const int L = (6 * lw + i) * 64 + lane, k = L / 12, c = L % 12;
      const int kk = k & 15, d = (kk & 3) + 8 * ((kk >> 2) & 1) + 4 * (kk >> 3);
      w0[i] = (18 * (k >> 4) + d) * 1152 + c * 16;
      w1[i] = (18 * (k >> 1) + 16 + (k & 1)) * 1152 + c * 16;
    }
    const char* hs = ws + WS_H + rt * 128;
    const char* os = ws + WS_O + (size_t)(2 * rt + lw) * 36864;
    const char* wos = (const char*)wo + cb * 192;
    int slotb = 0;
    auto issue = [&](int c) {
      const unsigned bb = lds0 + slotb * O_BUF;
      slotb = slotb == O_NB - 1 ? 0 : slotb + 1;
      if (c < 18) {
        const char* cs = (const char*)(c < 9 ? cre : cim) + cb * 96 * 1152 + (c < 9 ? c : c - 9) * 128;
#pragma unroll
        for (int i = 0; i < 4; ++i) dma16(hs + (size_t)c * 64 * 4096, ho[i], bb + (4 * lw + i) * 1024);
#pragma unroll
        for (int i = 0; i < 6; ++i) dma16(cs, co[i], bb + O_AB + (6 * lw + i) * 1024);
      } else {
        const int e = c - 18;
#pragma unroll
        for (int i = 0; i < 4; ++i) dma16(os + e * 4096 + i * 1024, lane * 16, bb + (4 * lw + i) * 1024);
#pragma unroll
        for (int i = 0; i < 6; ++i) dma16(wos + (e < 8 ? e * 72 * 1152 : 0), e < 8 ? w0[i] : w1[i], bb + O_AB + (6 * lw + i) * 1024);
      }
    };
#pragma unroll 1
    for (int c = 0; c < 6; ++c) issue(c);
#pragma unroll 1
    for (int c = 0; c < 21; ++c) { WAIT_VM(50); BARRIER(); issue(c + 6); }
    WAIT_VM(50); BARRIER();
    WAIT_VM(40); BARRIER();
    WAIT_VM(30); BARRIER();
    WAIT_VM(20); BARRIER();
    WAIT_VM(10); BARRIER();
    WAIT_VM(0); BARRIER();
    return;
  }
  const int r = lane & 31, h = lane >> 5, ti = w & 1, tj = w >> 1;
  const int blk = (lane >> 4) & 1, q = (lane & 15) >> 2, p = lane & 3;
  lds_cptr lds = (lds_cptr)smem;
  const int tok0 = rt * 64 + ti * 32, col = cb * 96 + tj * 32 + r;
  u16 xv[16];
#pragma unroll
  for (int i = 0; i < 16; ++i) xv[i] = xb[(tok0 + crow(i, h)) * DM + col];
  const float dsk = bf_val(dskip[col]);
  int boff = 0;
  {
    const int ha = (8 * h + q) * 128 + (((4 * ti + 2 * blk + (p >> 1)) ^ ((q >> 1) << 2)) << 4) + 8 * (p & 1);
    int ca[4];
#pragma unroll
    for (int ks = 0; ks < 4; ++ks) ca[ks] = O_AB + (32 * tj + r) * 128 + (((2 * ks + h) ^ ((r >> 1) & 7)) << 4);
    f32x16 acc = {};
#pragma unroll 1
    for (int c = 0; c < 18; ++c) {
      BARRIER();
      if (c == 0) STAMP(2, 1);
      bf16x8 af[4], bf[4];
#pragma unroll
      for (int ks = 0; ks < 4; ++ks) { af[ks] = lds_frag_tr(lds + boff + ha + ks * 2048, 512); bf[ks] = lds_frag(lds + boff + ca[ks]); }
#pragma unroll
      for (int ks = 0; ks < 4; ++ks) acc = MFMA32(af[ks], bf[ks], acc);
      boff = boff == (O_NB - 1) * O_BUF ? 0 : boff + O_BUF;
    }
    STAMP(2, 2);
#pragma unroll
    for (int i = 0; i < 16; ++i) {
      const float skip = bf_val(__builtin_bit_cast(u16, (__bf16)(bf_val(xv[i]) * dsk)));
      out[(size_t)(tok0 + crow(i, h)) * 1152 + 576 + col] = acc[i] + skip;
    }
  }
  {
    const int wa = O_AB + (8 * h + q) * 192 + (4 * tj + 2 * blk + (p >> 1)) * 16 + 8 * (p & 1);
    f32x16 acc = {};
#pragma unroll 1
    for (int c = 0; c < 8; ++c) {
      BARRIER();
      bf16x8 af[4], bf[4];
#pragma unroll
      for (int ks = 0; ks < 4; ++ks) { af[ks] = lds_frag(lds + boff + ti * 4096 + ks * 1024 + lane * 16); bf[ks] = lds_frag_tr(lds + boff + wa + ks * 3072, 768); }
#pragma unroll
      for (int ks = 0; ks < 4; ++ks) acc = MFMA32(af[ks], bf[ks], acc);
      boff = boff == (O_NB - 1) * O_BUF ? 0 : boff + O_BUF;
    }
    BARRIER();
#pragma unroll
    for (int ks = 0; ks < 4; ++ks) {
      typedef const __attribute__((address_space(3))) unsigned* up;
      const up s = (up)(lds + boff + ti * 4096 + (8 * ks + 4 * h) * 128 + r * 4);
      const u32x4 a = {s[0], s[32], s[64], s[96]};
      acc = MFMA32(__builtin_bit_cast(bf16x8, a), lds_frag_tr(lds + boff + wa + ks * 3072, 768), acc);
    }
    STAMP(2, 3);
#pragma unroll
    for (int i = 0; i < 16; ++i) out[(size_t)(tok0 + crow(i, h)) * 1152 + col] = acc[i];
  }
  WAIT_VM(0);
  STAMP(2, 4);
}

__global__ void k_report(const char* ws) {
  __shared__ float res[64];
  const unsigned long long* h = (const unsigned long long*)(ws + WS_DIAG);
  unsigned long long g0 = ~0ull;
  for (int b = 0; b < 256; ++b) { unsigned long long x = h[((size_t)b * 8 * 16) * 2 + 1]; g0 = x < g0 ? x : g0; }
  const int t = threadIdx.x;
  if (t < 18) {
    const int k = t / 6, i = t % 6;
    const int nb = k == 2 ? 192 : 256;
    float mn = 1e30f, mx = 0, av = 0; int n = 0;
    const int w = (k == 1 && i >= 4) ? 4 : 0;
    for (int b = 0; b < nb; ++b) {
      if (k == 0 && (b & 1) && (b >> 3) >= 28) continue;
      const float v = (float)(long long)(h[((((size_t)k * 256 + b) * 8 + w) * 16 + i) * 2 + 1] - g0) / 100.f;
      mn = fminf(mn, v); mx = fmaxf(mx, v); av += v; ++n;
    }
    res[t * 3] = mn; res[t * 3 + 1] = av / n; res[t * 3 + 2] = mx;
  }
  __syncthreads();
  if (t == 0) {
    printf("P %.1f/%.1f %.1f/%.1f %.1f/%.1f %.1f/%.1f %.1f/%.1f | A %.1f/%.1f/%.1f %.1f/%.1f %.1f/%.1f %.1f/%.1f %.1f/%.1f %.1f/%.1f | O %.1f/%.1f/%.1f %.1f/%.1f %.1f/%.1f %.1f/%.1f %.1f/%.1f\n",
      res[1], res[2], res[4], res[5], res[7], res[8], res[10], res[11], res[13], res[14],
      res[18], res[19], res[20], res[22], res[23], res[25], res[26], res[28], res[29], res[31], res[32], res[34], res[35],
      res[36], res[37], res[38], res[40], res[41], res[43], res[44], res[46], res[47], res[49], res[50]);
  }
}
extern "C" void kernel_launch(void* const* d_in, const int* in_sizes, int n_in, void* d_out, int out_size,
                              void* d_ws, size_t ws_size, hipStream_t stream) {
  const u16* xa = (const u16*)d_in[0];
  const u16* wqkv = (const u16*)d_in[1];
  const u16* wo = (const u16*)d_in[2];
  const u16* xb = (const u16*)d_in[3];
  const u16* lre = (const u16*)d_in[4];
  const u16* lim = (const u16*)d_in[5];
  const u16* bre = (const u16*)d_in[6];
  const u16* bim = (const u16*)d_in[7];
  const u16* cre = (const u16*)d_in[8];
  const u16* cim = (const u16*)d_in[9];
  const u16* logdt = (const u16*)d_in[10];
  const u16* dskip = (const u16*)d_in[11];
  char* ws = (char*)d_ws;
  constexpr int L1 = 4 * P_BUF, L2 = A_END + 2048 + 1024, L3 = O_NB * O_BUF;
  (void)hipFuncSetAttribute((const void*)k_proj, hipFuncAttributeMaxDynamicSharedMemorySize, L1);
  (void)hipFuncSetAttribute((const void*)k_attn, hipFuncAttributeMaxDynamicSharedMemorySize, L2);
  (void)hipFuncSetAttribute((const void*)k_out, hipFuncAttributeMaxDynamicSharedMemorySize, L3);
  k_proj<<<256, 512, L1, stream>>>(xa, wqkv, xb, bre, bim, lre, lim, logdt, ws);
  k_attn<<<256, 512, L2, stream>>>(ws);
  k_out<<<192, 512, L3, stream>>>(xb, cre, cim, dskip, wo, ws, (float*)d_out);
  k_report<<<1, 64, 0, stream>>>(ws);
}
```

```cpp
#include <hip/hip_runtime.h>
#include <hip/hip_bf16.h>

typedef unsigned short u16;
typedef __attribute__((ext_vector_type(8))) __bf16 bf16x8;
typedef __attribute__((ext_vector_type(16))) __bf16 bf16x16;
typedef __attribute__((ext_vector_type(2))) __bf16 bf16x2;
typedef __attribute__((ext_vector_type(4))) short s16x4;
typedef __attribute__((ext_vector_type(2))) float f32x2;
typedef __attribute__((ext_vector_type(8))) float f32x8;
typedef __attribute__((ext_vector_type(16))) float f32x16;
typedef __attribute__((ext_vector_type(32))) float f32x32;
typedef __attribute__((ext_vector_type(2))) unsigned u32x2;
typedef __attribute__((ext_vector_type(4))) unsigned u32x4;
typedef __attribute__((address_space(3))) const char* lds_cptr;
typedef __attribute__((address_space(3))) char* lds_ptr;

#define DI __device__ __forceinline__
#define SBAR() __builtin_amdgcn_sched_barrier(0)
#define PIN(x) asm volatile("" : "+v"(x))

constexpr int SEQ = 2048, DM = 576, NH = 32;

constexpr size_t WS_QM = 0;
constexpr size_t WS_QR = WS_QM + 32 * 65536;
constexpr size_t WS_KM = WS_QR + 16 * 16384;
constexpr size_t WS_KR = WS_KM + 32 * 65536;
constexpr size_t WS_V = WS_KR + 16 * 16384;
constexpr size_t WS_U = WS_V + 32 * 77824;
constexpr size_t WS_H = WS_U + (size_t)576 * 8192;
constexpr size_t WS_O = WS_H + (size_t)1152 * 4096;
constexpr size_t WS_TAB = WS_O + (size_t)64 * 36864;

DI unsigned pack_bf16(float lo, float hi) {
  f32x2 v = {lo, hi};
  return __builtin_bit_cast(unsigned, __builtin_convertvector(v, bf16x2));
}
DI float bf_lo(unsigned x) { return __uint_as_float(x << 16); }
DI float bf_hi(unsigned x) { return __uint_as_float(x & 0xffff0000u); }
DI float bf_val(u16 x) { return __uint_as_float((unsigned)x << 16); }

DI void dma16(const void* base, unsigned voff, unsigned lds_dst) {
  asm volatile("s_mov_b32 m0, %2\n\ts_nop 0\n\tglobal_load_lds_dwordx4 %0, %1" ::"v"(voff), "s"(base), "s"(lds_dst) : "memory");
}
#define WAIT_VM(n) asm volatile("s_waitcnt vmcnt(" #n ")" ::: "memory")
#define BARRIER() asm volatile("s_waitcnt lgkmcnt(0)\n\ts_barrier" ::: "memory")

DI bf16x8 lds_frag(lds_cptr p) { return *(const __attribute__((address_space(3))) bf16x8*)p; }
DI bf16x8 lds_frag_tr(lds_cptr p, int second) {
  typedef __attribute__((address_space(3))) s16x4* tp;
  const s16x4 lo = __builtin_amdgcn_ds_read_tr16_b64_v4i16((tp)p);
  const s16x4 hi = __builtin_amdgcn_ds_read_tr16_b64_v4i16((tp)(p + second));
  return __builtin_bit_cast(bf16x8, __builtin_shufflevector(lo, hi, 0, 1, 2, 3, 4, 5, 6, 7));
}
#define MFMA32(a, b, c) __builtin_amdgcn_mfma_f32_32x32x16_bf16((a), (b), (c), 0, 0, 0)

#define STAMP(k, i)
DI int crow(int reg, int h) { return (reg & 3) + 8 * (reg >> 2) + 4 * h; }

constexpr int P_BUF = 40960, P_XB = 16384;

template <int MODE>
DI void proj_compute(lds_cptr lds, int w, int lane, f32x16 (&acc)[2][3], const char* ws) {
  const int r = lane & 31, h = lane >> 5, tw = w & 1, cw = w >> 1;
  int xa[4], wa[4];
#pragma unroll
  for (int ks = 0; ks < 4; ++ks) xa[ks] = (64 * tw + r) * 128 + (((2 * ks + h) ^ ((r >> 1) & 7)) << 4);
  if (MODE == 2) {
    const int row = (r & 1) * 96 + 48 * cw + (r >> 1);
    const int f = ((row >> 1) & 7) ^ (r & 1);
#pragma unroll
    for (int ks = 0; ks < 4; ++ks) wa[ks] = P_XB + row * 128 + (((2 * ks + h) ^ f) << 4);
  } else {
    const int blk = (lane >> 4) & 1, q = (lane & 15) >> 2, p = lane & 3;
#pragma unroll
    for (int j = 0; j < 3; ++j)
      wa[j] = P_XB + (8 * h + q) * 384 + (((12 * cw + 4 * j + 2 * blk + (p >> 1)) ^ ((q >> 1) << 2)) << 4) + 8 * (p & 1);
    wa[3] = 0;
  }
  bf16x8 xf[2][2], wf[2][3];
  auto load = [&](int s, int boff, int ks) {
#pragma unroll
    for (int i = 0; i < 2; ++i) xf[s][i] = lds_frag(lds + boff + xa[ks] + i * 4096);
#pragma unroll
    for (int j = 0; j < 3; ++j) {
      if (MODE == 2) wf[s][j] = lds_frag(lds + boff + wa[ks] + j * 2048);
      else wf[s][j] = lds_frag_tr(lds + boff + wa[j] + ks * 6144, 1536);
    }
  };
  auto mma = [&](int s) {
#pragma unroll
    for (int j = 0; j < 3; ++j)
#pragma unroll
      for (int i = 0; i < 2; ++i) {
        if (MODE == 1) acc[i][j] = MFMA32(xf[s][i], wf[s][j], acc[i][j]);
        else acc[i][j] = MFMA32(wf[s][j], xf[s][i], acc[i][j]);
      }
  };
  BARRIER();
  STAMP(0, 1);
  load(0, 0, 0);
  int boff = 0;
#pragma unroll 1
  for (int c = 0; c < 9; ++c) {
    load(1, boff, 1); SBAR(); mma(0); SBAR();
    load(0, boff, 2); SBAR(); mma(1); SBAR();
    load(1, boff, 3); SBAR(); mma(0); SBAR();
    boff = (boff == 3 * P_BUF) ? 0 : boff + P_BUF;
    if (c < 8) { BARRIER(); load(0, boff, 0); }
    SBAR(); mma(1); SBAR();
  }
  STAMP(0, 2);
}

DI void ssm_table(const u16* lre, const u16* lim, const u16* logdt, float* tab, int p) {
  const float dt = bf_val(__builtin_bit_cast(u16, (__bf16)expf(bf_val(logdt[p]))));
  const float lr = bf_val(lre[p]), li = bf_val(lim[p]);
  const float mag = expf(dt * lr);
  const float ang = dt * li;
  const float ar = mag * cosf(ang), ai = mag * sinf(ang);
  const double nr = (double)ar - 1.0, ni = ai, den = (double)lr * lr + (double)li * li;
  float* t = tab + p * 16;
  t[0] = ar; t[1] = ai;
  t[2] = (float)((nr * lr + ni * li) / den);
  t[3] = (float)((ni * lr - nr * li) / den);
  double pr = ar, pi = ai;
  for (int i = 0; i < 5; ++i) { const double n = pr * pr - pi * pi; pi = 2.0 * pr * pi; pr = n; }
  for (int i = 0; i < 6; ++i) {
    t[4 + 2 * i] = (float)pr; t[5 + 2 * i] = (float)pi;
    const double n = pr * pr - pi * pi; pi = 2.0 * pr * pi; pr = n;
  }
}

__global__ __launch_bounds__(512, 2) void k_proj(const u16* __restrict__ xa, const u16* __restrict__ wqkv,
                                                 const u16* __restrict__ xb, const u16* __restrict__ bre,
                                                 const u16* __restrict__ bim, const u16* __restrict__ lre,
                                                 const u16* __restrict__ lim, const u16* __restrict__ logdt,
                                                 char* __restrict__ ws) {
  extern __shared__ __attribute__((aligned(16))) char smem[];
  const int xcd = blockIdx.x & 7, slot = blockIdx.x >> 3;
  const int cb = (xcd & 1) * 8 + (slot >> 2), rt = (xcd >> 1) * 4 + (slot & 3);
  const int tid = threadIdx.x, lane = tid & 63;
  const int w = __builtin_amdgcn_readfirstlane(tid >> 6);
  STAMP(0, 0);
  if (cb == 15) {
    if (tid < 36) ssm_table(lre, lim, logdt, (float*)(ws + WS_TAB), rt * 36 + tid);
    return;
  }
  const unsigned lds0 = (unsigned)(size_t)(lds_ptr)smem;
  if (w >= 4) {
    const int lw = w - 4;
    unsigned xo[4], wo[6];
#pragma unroll
    for (int i = 0; i < 4; ++i) {
      const int row = 8 * (4 * lw + i) + (lane >> 3);
      xo[i] = row * 1152 + (((lane & 7) ^ ((row >> 1) & 7)) << 4);
    }
    const char *xs, *wsrc;
    unsigned wstep;
    if (cb < 9) {
      xs = (const char*)xa;
      wsrc = (const char*)wqkv + cb * 384;
      wstep = 64 * 3456;
#pragma unroll
      for (int i = 0; i < 6; ++i) {
        const int L = (6 * lw + i) * 64 + lane, k = L / 24, c = L % 24;
        wo[i] = k * 3456 + ((c ^ (((k >> 1) & 1) << 2)) << 4);
      }
    } else {
      xs = (const char*)xb;
      wsrc = (const char*)(lw < 2 ? bre : bim) + (cb - 9) * 96 * 1152;
      wstep = 128;
#pragma unroll
      for (int i = 0; i < 6; ++i) {
        const int row = 8 * (6 * (lw & 1) + i) + (lane >> 3);
        wo[i] = row * 1152 + (((lane & 7) ^ ((row >> 1) & 7) ^ (lw >> 1)) << 4);
      }
    }
    xs += rt * 128 * 1152;
    auto issue = [&](int c) {
      const unsigned b = lds0 + (c & 3) * P_BUF;
#pragma unroll
      for (int i = 0; i < 4; ++i) dma16(xs + c * 128, xo[i], b + (4 * lw + i) * 1024);
#pragma unroll
      for (int i = 0; i < 6; ++i) dma16(wsrc + (size_t)c * wstep, wo[i], b + P_XB + (6 * lw + i) * 1024);
    };
#pragma unroll 1
    for (int c = 0; c < 4; ++c) issue(c);
    WAIT_VM(30); BARRIER();
#pragma unroll 1
    for (int c = 1; c < 6; ++c) { WAIT_VM(20); BARRIER(); issue(c + 3); }
    WAIT_VM(20); BARRIER();
    WAIT_VM(10); BARRIER();
    WAIT_VM(0); BARRIER();
    return;
  }
  const int r = lane & 31, h = lane >> 5, tw = w & 1, cw = w >> 1;
  f32x16 acc[2][3];
#pragma unroll
  for (int i = 0; i < 2; ++i)
#pragma unroll
    for (int j = 0; j < 3; ++j) acc[i][j] = f32x16{};
  const int tile0 = rt * 4 + tw * 2;
  if (cb < 6) {
    proj_compute<0>((lds_cptr)smem, w, lane, acc, ws);
    const bool isq = cb < 3;
    const float sc = isq ? 0.23570226039551584f * 1.4426950408889634f : 1.0f;
    const int sl = isq ? r : ((r & 19) | ((r & 4) << 1) | ((r & 8) >> 1));
    char* mainb = ws + (isq ? WS_QM : WS_KM);
    constexpr unsigned REM = WS_QR - WS_QM;
#pragma unroll
    for (int j = 0; j < 3; ++j)
#pragma unroll
      for (int m = 0; m < 4; ++m) {
        const int G = (isq ? cb : cb - 3) * 48 + cw * 24 + j * 8 + 2 * m + h;
        const int pair = G / 9, pos = G - 9 * pair;
        const int qd = pos > 4 ? pos - 5 : pos;
        const unsigned mo = (2 * pair + (pos > 4)) * 65536 + tile0 * 1024 + ((qd >> 1) * 32 + sl) * 16 + (qd & 1) * 8;
        const unsigned ro = REM + pair * 16384 + (tile0 * 32 + sl) * 8;
        const unsigned off = pos == 4 ? ro : mo, ts = pos == 4 ? 256 : 1024;
#pragma unroll
        for (int i = 0; i < 2; ++i) {
          const u32x2 v = {pack_bf16(acc[i][j][4 * m] * sc, acc[i][j][4 * m + 1] * sc),
                           pack_bf16(acc[i][j][4 * m + 2] * sc, acc[i][j][4 * m + 3] * sc)};
          *(u32x2*)(mainb + off + i * ts) = v;
        }
      }
  } else if (cb < 9) {
    proj_compute<1>((lds_cptr)smem, w, lane, acc, ws);
#pragma unroll
    for (int j = 0; j < 3; ++j) {
      const int n = (cb - 6) * 192 + cw * 96 + j * 32 + r, head = n / 18, d = n - 18 * head;
      char* dst = ws + WS_V + head * 77824 + tile0 * 1216 + (h * 19 + d) * 16;
#pragma unroll
      for (int i = 0; i < 2; ++i)
#pragma unroll
        for (int s = 0; s < 2; ++s) {
          const f32x16& a = acc[i][j];
          const u32x4 v = {pack_bf16(a[8 * s], a[8 * s + 1]), pack_bf16(a[8 * s + 2], a[8 * s + 3]),
                           pack_bf16(a[8 * s + 4], a[8 * s + 5]), pack_bf16(a[8 * s + 6], a[8 * s + 7])};
          *(u32x4*)(dst + i * 1216 + s * 608) = v;
        }
      if (d == 17) {
        const u32x4 ones = {0x3f803f80u, 0x3f803f80u, 0x3f803f80u, 0x3f803f80u};
#pragma unroll
        for (int i = 0; i < 4; ++i) *(u32x4*)(dst + 16 + i * 608) = ones;
      }
    }
  } else {
    proj_compute<2>((lds_cptr)smem, w, lane, acc, ws);
    char* dst = ws + WS_U + ((cb - 9) * 96 + cw * 48 + 2 * h) * 8192 + (tile0 * 32 + r) * 4;
#pragma unroll
    for (int j = 0; j < 3; ++j)
#pragma unroll
      for (int t = 0; t < 8; ++t)
#pragma unroll
        for (int i = 0; i < 2; ++i)
          *(unsigned*)(dst + (16 * j + 4 * (t >> 1) + (t & 1)) * 8192 + i * 128) = pack_bf16(acc[i][j][2 * t], acc[i][j][2 * t + 1]);
  }
  STAMP(0, 3);
  STAMP(0, 4);
}

constexpr int A_K = 0, A_V = 65536, A_R = A_V + 77824, A_END = A_R + 16384;

DI f32x2 cfma(f32x2 a, f32x2 x, f32x2 c) {
  return f32x2{__builtin_fmaf(a[0], x[0], __builtin_fmaf(-a[1], x[1], c[0])),
               __builtin_fmaf(a[0], x[1], __builtin_fmaf(a[1], x[0], c[1]))};
}

DI void ssm_scan(char* ws, int p, int lane, const u32x4 (&u)[8]) {
  const float* t = (const float*)(ws + WS_TAB) + p * 16;
  const f32x2 a = {t[0], t[1]};
  f32x2 e = {0.f, 0.f};
#pragma unroll
  for (int i = 0; i < 32; ++i) e = cfma(a, e, f32x2{bf_lo(u[i >> 2][i & 3]), bf_hi(u[i >> 2][i & 3])});
#pragma unroll
  for (int s = 0; s < 6; ++s) {
    const int d = 1 << s;
    f32x2 o = {__shfl_up(e[0], d), __shfl_up(e[1], d)};
    if (lane < d) o = f32x2{0.f, 0.f};
    e = cfma(f32x2{t[4 + 2 * s], t[5 + 2 * s]}, o, e);
  }
  f32x2 hs = {__shfl_up(e[0], 1), __shfl_up(e[1], 1)};
  if (lane == 0) hs = f32x2{0.f, 0.f};
  const float cr = 2.f * t[2], ci = 2.f * t[3];
  char* dre = ws + WS_H + (size_t)p * 4096 + lane * 64;
  char* dim = dre + (size_t)576 * 4096;
#pragma unroll
  for (int g = 0; g < 4; ++g) {
    float re[8], im[8];
#pragma unroll
    for (int i = 0; i < 8; ++i) {
      const unsigned x = u[2 * g + (i >> 2)][i & 3];
      hs = cfma(a, hs, f32x2{bf_lo(x), bf_hi(x)});
      re[i] = cr * hs[0] - ci * hs[1];
      im[i] = -cr * hs[1] - ci * hs[0];
    }
    *(u32x4*)(dre + g * 16) = u32x4{pack_bf16(re[0], re[1]), pack_bf16(re[2], re[3]), pack_bf16(re[4], re[5]), pack_bf16(re[6], re[7])};
    *(u32x4*)(dim + g * 16) = u32x4{pack_bf16(im[0], im[1]), pack_bf16(im[2], im[3]), pack_bf16(im[4], im[5]), pack_bf16(im[6], im[7])};
  }
}

DI f32x16 lo16(const f32x32& x) { return __builtin_shufflevector(x, x, 0, 1, 2, 3, 4, 5, 6, 7, 8, 9, 10, 11, 12, 13, 14, 15); }
DI f32x16 hi16(const f32x32& x) { return __builtin_shufflevector(x, x, 16, 17, 18, 19, 20, 21, 22, 23, 24, 25, 26, 27, 28, 29, 30, 31); }
DI bf16x16 p_lo(const f32x16& x) { return __builtin_bit_cast(bf16x16, __builtin_shufflevector(x, x, 0, 1, 2, 3, 4, 5, 6, 7)); }
DI bf16x16 p_hi(const f32x16& x) { return __builtin_bit_cast(bf16x16, __builtin_shufflevector(x, x, 8, 9, 10, 11, 12, 13, 14, 15)); }
#define PV(o, v, p) o = __builtin_amdgcn_smfmac_f32_32x32x32_bf16((v), (p), (o), sidx, 0, 0)
#define QKREM(kr, qr, blk) __builtin_amdgcn_mfma_f32_32x32x4bf16_1k((kr), (qr), f32x32{}, 1, (blk), 0)
#define EX(x, i) x[i] = __builtin_amdgcn_exp2f(x[i])

__global__ __launch_bounds__(512, 2) void k_attn(char* __restrict__ ws) {
  extern __shared__ __attribute__((aligned(16))) char smem[];
  const int b = blockIdx.x;
  const int head = 4 * (b & 7) + ((b >> 3) & 3), qb = b >> 5;
  const int tid = threadIdx.x, lane = tid & 63;
  const int w = __builtin_amdgcn_readfirstlane(tid >> 6);
  const int r = lane & 31, h = lane >> 5, qw = w & 3, kh = w >> 2;
  const unsigned lds0 = (unsigned)(size_t)(lds_ptr)smem;

  STAMP(1, 0);
  const bool scan = b < 192 && w < 3;
  const int chan = 3 * b + w;
  u32x4 u[8];
  if (scan) {
#pragma unroll
    for (int i = 0; i < 8; ++i) u[i] = *(const u32x4*)(ws + WS_U + (size_t)chan * 8192 + lane * 128 + i * 16);
  }
  const int qt = qb * 8 + qw * 2;
  const bf16x8 q0 = *(const bf16x8*)(ws + WS_QM + head * 65536 + qt * 1024 + lane * 16);
  const bf16x8 q1 = *(const bf16x8*)(ws + WS_QM + head * 65536 + qt * 1024 + 1024 + lane * 16);
  const unsigned qrw = *(const unsigned*)(ws + WS_QR + (head >> 1) * 16384 + (qt * 32 + lane) * 8 + (head & 1) * 4);
  {
    const unsigned vo = lane * 16;
#pragma unroll 1
    for (int i = w; i < 64; i += 8) dma16(ws + WS_KM + head * 65536 + i * 1024, vo, lds0 + A_K + i * 1024);
#pragma unroll 1
    for (int i = w; i < 76; i += 8) dma16(ws + WS_V + head * 77824 + i * 1024, vo, lds0 + A_V + i * 1024);
#pragma unroll 1
    for (int i = w; i < 16; i += 8) dma16(ws + WS_KR + (head >> 1) * 16384 + i * 1024, vo, lds0 + A_R + i * 1024);
  }
  STAMP(1, 1);
  if (scan) ssm_scan(ws, chan, lane, u);
  STAMP(1, 2);
  const u32x2 qru = (head & 1) ? u32x2{0u, qrw} : u32x2{qrw, 0u};
  const s16x4 qr = __builtin_bit_cast(s16x4, qru);
  WAIT_VM(0);
  BARRIER();
  STAMP(1, 3);

  const int vrow = r < 19 ? r : (r >= 20 && r < 28 ? 0 : 18);
  lds_cptr kp = (lds_cptr)smem + A_K + kh * 32768 + lane * 16;
  lds_cptr vp = (lds_cptr)smem + A_V + kh * 38912 + (h * 19 + vrow) * 16;
  lds_cptr rp = (lds_cptr)smem + A_R + kh * 8192 + lane * 8;
  typedef const __attribute__((address_space(3))) s16x4* rptr;
  int sidx = 0xDDDD;
  PIN(sidx);
  f32x16 o0 = {}, o1 = {};
  {
    f32x16 a0, a1, b0, b1;
    bf16x8 kf, va0, va1, vb0, vb1;
    s16x4 kr = *(rptr)rp;
    kf = lds_frag(kp);
    va0 = lds_frag(vp); va1 = lds_frag(vp + 608);
    { const f32x32 s = QKREM(kr, qr, 0); a0 = MFMA32(kf, q0, lo16(s)); a1 = MFMA32(kf, q1, hi16(s)); }
    vb1 = va1;
    b1 = f32x16{};
#define STEP(C0, C1, N0, N1, V0, V1, VN0, VP1, KOFF, VOFF, BLK, RLOAD)                                   \
  do {                                                                                                   \
    kf = lds_frag(kp + (KOFF)); RLOAD; SBAR();                                                           \
    EX(C0, 0); EX(C0, 1); PIN(C0); SBAR();                                                               \
    PV(o1, VP1, p_hi(N1)); SBAR();                                                                       \
    VN0 = lds_frag(vp + (VOFF)); VP1 = lds_frag(vp + (VOFF) + 608); SBAR();                              \
    EX(C0, 2); EX(C0, 3); PIN(C0); SBAR();                                                               \
    const f32x32 s_ = QKREM(kr, qr, BLK); SBAR();                                                        \
    EX(C0, 4); EX(C0, 5); EX(C0, 6); EX(C0, 7); EX(C0, 8); EX(C0, 9); PIN(C0); SBAR();                   \
    PV(o0, V0, p_lo(C0)); SBAR();                                                                        \
    EX(C0, 10); EX(C0, 11); EX(C0, 12); EX(C0, 13); EX(C0, 14); EX(C0, 15); PIN(C0);                     \
    EX(C1, 0); EX(C1, 1); PIN(C1); SBAR();                                                               \
    PV(o0, V1, p_hi(C0)); SBAR();                                                                        \
    EX(C1, 2); EX(C1, 3); EX(C1, 4); EX(C1, 5); PIN(C1); SBAR();                                         \
    N0 = MFMA32(kf, q0, lo16(s_)); SBAR();                                                               \
    EX(C1, 6); EX(C1, 7); EX(C1, 8); EX(C1, 9); PIN(C1); SBAR();                                         \
    PV(o1, V0, p_lo(C1)); SBAR();                                                                        \
    EX(C1, 10); EX(C1, 11); PIN(C1); SBAR();                                                             \
    N1 = MFMA32(kf, q1, hi16(s_)); SBAR();                                                               \
    EX(C1, 12); EX(C1, 13); EX(C1, 14); EX(C1, 15); PIN(C1); SBAR();                                     \
  } while (0)
#pragma unroll 1
    for (int t = 0; t < 16; ++t) {
      STEP(a0, a1, b0, b1, va0, va1, vb0, vb1, 1024, 1216, 1, (void)0);
      STEP(b0, b1, a0, a1, vb0, vb1, va0, va1, 2048, 2432, 0, kr = *(rptr)(rp + 512));
      kp += 2048; vp += 2432; rp += 512;
    }
    PV(o1, vb1, p_hi(b1));
  }
  STAMP(1, 4);

  auto combine = [&](f32x16& mine) -> float {
    BARRIER();
    __attribute__((address_space(3))) float* xch = (__attribute__((address_space(3))) float*)((lds_ptr)smem + A_END);
    (void)xch;
    __attribute__((address_space(3))) float* buf = (__attribute__((address_space(3))) float*)((lds_ptr)smem + A_K);
    const f32x16 give = kh ? o0 : o1;
    mine = kh ? o1 : o0;
#pragma unroll
    for (int i = 0; i < 13; ++i) buf[(w * 13 + i) * 64 + lane] = give[i];
    BARRIER();
#pragma unroll
    for (int i = 0; i < 13; ++i) mine[i] += buf[((w ^ 4) * 13 + i) * 64 + lane];
    return h ? mine[12] : mine[10];
  };
  f32x16 om;
  float l = combine(om);
  bool ok = l >= 0x1p-64f && l <= 0x1p64f;
#pragma unroll
  for (int i = 0; i < 8; ++i) ok = ok && __builtin_fabsf(om[i]) < __builtin_inff();
  ok = ok && (h || (__builtin_fabsf(om[8]) < __builtin_inff() && __builtin_fabsf(om[9]) < __builtin_inff()));
  if (__syncthreads_or(!ok)) {
#pragma unroll 1
    for (int i = w; i < 64; i += 8) dma16(ws + WS_KM + head * 65536 + i * 1024, lane * 16, lds0 + A_K + i * 1024);
    WAIT_VM(0);
    BARRIER();
    kp = (lds_cptr)smem + A_K + kh * 32768 + lane * 16;
    vp = (lds_cptr)smem + A_V + kh * 38912 + (h * 19 + vrow) * 16;
    rp = (lds_cptr)smem + A_R + kh * 8192 + r * 8;
    float m0 = -__builtin_inff(), m1 = -__builtin_inff();
#pragma unroll 1
    for (int t = 0; t < 32; ++t) {
      const bf16x8 kf = lds_frag(kp + t * 1024);
      const f32x32 s = QKREM(*(rptr)(rp + t * 256), qr, 0);
      const f32x16 s0 = MFMA32(kf, q0, lo16(s)), s1 = MFMA32(kf, q1, hi16(s));
#pragma unroll
      for (int i = 0; i < 16; ++i) { m0 = fmaxf(m0, s0[i]); m1 = fmaxf(m1, s1[i]); }
    }
    m0 = fmaxf(m0, __shfl_xor(m0, 32));
    m1 = fmaxf(m1, __shfl_xor(m1, 32));
    __attribute__((address_space(3))) float* xch = (__attribute__((address_space(3))) float*)((lds_ptr)smem + A_END);
    xch[w * 64 + lane] = h ? m1 : m0;
    BARRIER();
    m0 = fmaxf(m0, xch[(w ^ 4) * 64 + r]);
    m1 = fmaxf(m1, xch[(w ^ 4) * 64 + 32 + r]);
    o0 = f32x16{}; o1 = f32x16{};
#pragma unroll 1
    for (int t = 0; t < 32; ++t) {
      const bf16x8 kf = lds_frag(kp + t * 1024);
      const f32x32 s = QKREM(*(rptr)(rp + t * 256), qr, 0);
      f32x16 s0 = MFMA32(kf, q0, lo16(s)), s1 = MFMA32(kf, q1, hi16(s));
#pragma unroll
      for (int i = 0; i < 16; ++i) { s0[i] = __builtin_amdgcn_exp2f(s0[i] - m0); s1[i] = __builtin_amdgcn_exp2f(s1[i] - m1); }
      const bf16x8 v0 = lds_frag(vp + t * 1216), v1 = lds_frag(vp + t * 1216 + 608);
      PV(o0, v0, p_lo(s0)); PV(o0, v1, p_hi(s0));
      PV(o1, v0, p_lo(s1)); PV(o1, v1, p_hi(s1));
    }
    l = combine(om);
  }
  const float inv = 1.0f / l;
  char* od = ws + WS_O + (size_t)(qt + kh) * 36864;
  *(u32x4*)(od + head * 1024 + lane * 16) =
      u32x4{pack_bf16(om[0] * inv, om[1] * inv), pack_bf16(om[2] * inv, om[3] * inv),
            pack_bf16(om[4] * inv, om[5] * inv), pack_bf16(om[6] * inv, om[7] * inv)};
  if (h == 0) *(unsigned*)(od + 32768 + head * 128 + r * 4) = pack_bf16(om[8] * inv, om[9] * inv);
  STAMP(1, 5);
}

constexpr int O_BUF = 20480, O_AB = 8192, O_NB = 7;

__global__ __launch_bounds__(512, 2) void k_out(const u16* __restrict__ xb, const u16* __restrict__ cre,
                                                const u16* __restrict__ cim, const u16* __restrict__ dskip,
                                                const u16* __restrict__ wo, const char* __restrict__ ws,
                                                float* __restrict__ out) {
  extern __shared__ __attribute__((aligned(16))) char smem[];
  const int xcd = blockIdx.x & 7, slot = blockIdx.x >> 3;
  const int rt = (xcd >> 1) * 8 + (slot & 7), cb = (xcd & 1) * 3 + (slot >> 3);
  const int tid = threadIdx.x, lane = tid & 63;
  const int w = __builtin_amdgcn_readfirstlane(tid >> 6);
  const unsigned lds0 = (unsigned)(size_t)(lds_ptr)smem;
  STAMP(2, 0);
  if (w >= 6) {
    const int lw = w - 6;
    unsigned ho[4], co[6], w0[6], w1[6];
#pragma unroll
    for (int i = 0; i < 4; ++i) {
      const int k = 8 * (4 * lw + i) + (lane >> 3);
      ho[i] = k * 4096 + (((lane & 7) ^ (((k >> 1) & 1) << 2)) << 4);
    }
#pragma unroll
    for (int i = 0; i < 6; ++i) {
      const int row = 8 * (6 * lw + i) + (lane >> 3);
      co[i] = row * 1152 + (((lane & 7) ^ ((row >> 1) & 7)) << 4);
      const int L = (6 * lw + i) * 64 + lane, k = L / 12, c = L % 12;
      const int kk = k & 15, d = (kk & 3) + 8 * ((kk >> 2) & 1) + 4 * (kk >> 3);
      w0[i] = (18 * (k >> 4) + d) * 1152 + c * 16;
      w1[i] = (18 * (k >> 1) + 16 + (k & 1)) * 1152 + c * 16;
    }
    const char* hs = ws + WS_H + rt * 128;
    const char* os = ws + WS_O + (size_t)(2 * rt + lw) * 36864;
    const char* wos = (const char*)wo + cb * 192;
    int slotb = 0;
    auto issue = [&](int c) {
      const unsigned bb = lds0 + slotb * O_BUF;
      slotb = slotb == O_NB - 1 ? 0 : slotb + 1;
      if (c < 18) {
        const char* cs = (const char*)(c < 9 ? cre : cim) + cb * 96 * 1152 + (c < 9 ? c : c - 9) * 128;
#pragma unroll
        for (int i = 0; i < 4; ++i) dma16(hs + (size_t)c * 64 * 4096, ho[i], bb + (4 * lw + i) * 1024);
#pragma unroll
        for (int i = 0; i < 6; ++i) dma16(cs, co[i], bb + O_AB + (6 * lw + i) * 1024);
      } else {
        const int e = c - 18;
#pragma unroll
        for (int i = 0; i < 4; ++i) dma16(os + e * 4096 + i * 1024, lane * 16, bb + (4 * lw + i) * 1024);
#pragma unroll
        for (int i = 0; i < 6; ++i) dma16(wos + (e < 8 ? e * 72 * 1152 : 0), e < 8 ? w0[i] : w1[i], bb + O_AB + (6 * lw + i) * 1024);
      }
    };
#pragma unroll 1
    for (int c = 0; c < 6; ++c) issue(c);
#pragma unroll 1
    for (int c = 0; c < 21; ++c) { WAIT_VM(50); BARRIER(); issue(c + 6); }
    WAIT_VM(50); BARRIER();
    WAIT_VM(40); BARRIER();
    WAIT_VM(30); BARRIER();
    WAIT_VM(20); BARRIER();
    WAIT_VM(10); BARRIER();
    WAIT_VM(0); BARRIER();
    return;
  }
  const int r = lane & 31, h = lane >> 5, ti = w & 1, tj = w >> 1;
  const int blk = (lane >> 4) & 1, q = (lane & 15) >> 2, p = lane & 3;
  lds_cptr lds = (lds_cptr)smem;
  const int tok0 = rt * 64 + ti * 32, col = cb * 96 + tj * 32 + r;
  u16 xv[16];
#pragma unroll
  for (int i = 0; i < 16; ++i) xv[i] = xb[(tok0 + crow(i, h)) * DM + col];
  const float dsk = bf_val(dskip[col]);
  int boff = 0;
  {
    const int ha = (8 * h + q) * 128 + (((4 * ti + 2 * blk + (p >> 1)) ^ ((q >> 1) << 2)) << 4) + 8 * (p & 1);
    int ca[4];
#pragma unroll
    for (int ks = 0; ks < 4; ++ks) ca[ks] = O_AB + (32 * tj + r) * 128 + (((2 * ks + h) ^ ((r >> 1) & 7)) << 4);
    f32x16 acc = {};
#pragma unroll 1
    for (int c = 0; c < 18; ++c) {
      BARRIER();
      bf16x8 af[4], bf[4];
#pragma unroll
      for (int ks = 0; ks < 4; ++ks) { af[ks] = lds_frag_tr(lds + boff + ha + ks * 2048, 512); bf[ks] = lds_frag(lds + boff + ca[ks]); }
#pragma unroll
      for (int ks = 0; ks < 4; ++ks) acc = MFMA32(af[ks], bf[ks], acc);
      boff = boff == (O_NB - 1) * O_BUF ? 0 : boff + O_BUF;
    }
    STAMP(2, 2);
#pragma unroll
    for (int i = 0; i < 16; ++i) {
      const float skip = bf_val(__builtin_bit_cast(u16, (__bf16)(bf_val(xv[i]) * dsk)));
      out[(size_t)(tok0 + crow(i, h)) * 1152 + 576 + col] = acc[i] + skip;
    }
  }
  {
    const int wa = O_AB + (8 * h + q) * 192 + (4 * tj + 2 * blk + (p >> 1)) * 16 + 8 * (p & 1);
    f32x16 acc = {};
#pragma unroll 1
    for (int c = 0; c < 8; ++c) {
      BARRIER();
      bf16x8 af[4], bf[4];
#pragma unroll
      for (int ks = 0; ks < 4; ++ks) { af[ks] = lds_frag(lds + boff + ti * 4096 + ks * 1024 + lane * 16); bf[ks] = lds_frag_tr(lds + boff + wa + ks * 3072, 768); }
#pragma unroll
      for (int ks = 0; ks < 4; ++ks) acc = MFMA32(af[ks], bf[ks], acc);
      boff = boff == (O_NB - 1) * O_BUF ? 0 : boff + O_BUF;
    }
    BARRIER();
#pragma unroll
    for (int ks = 0; ks < 4; ++ks) {
      typedef const __attribute__((address_space(3))) unsigned* up;
      const up s = (up)(lds + boff + ti * 4096 + (8 * ks + 4 * h) * 128 + r * 4);
      const u32x4 a = {s[0], s[32], s[64], s[96]};
      acc = MFMA32(__builtin_bit_cast(bf16x8, a), lds_frag_tr(lds + boff + wa + ks * 3072, 768), acc);
    }
    STAMP(2, 3);
#pragma unroll
    for (int i = 0; i < 16; ++i) out[(size_t)(tok0 + crow(i, h)) * 1152 + col] = acc[i];
  }
  STAMP(2, 4);
}

extern "C" void kernel_launch(void* const* d_in, const int* in_sizes, int n_in, void* d_out, int out_size,
                              void* d_ws, size_t ws_size, hipStream_t stream) {
  const u16* xa = (const u16*)d_in[0];
  const u16* wqkv = (const u16*)d_in[1];
  const u16* wo = (const u16*)d_in[2];
  const u16* xb = (const u16*)d_in[3];
  const u16* lre = (const u16*)d_in[4];
  const u16* lim = (const u16*)d_in[5];
  const u16* bre = (const u16*)d_in[6];
  const u16* bim = (const u16*)d_in[7];
  const u16* cre = (const u16*)d_in[8];
  const u16* cim = (const u16*)d_in[9];
  const u16* logdt = (const u16*)d_in[10];
  const u16* dskip = (const u16*)d_in[11];
  char* ws = (char*)d_ws;
  constexpr int L1 = 4 * P_BUF, L2 = A_END + 2048 + 1024, L3 = O_NB * O_BUF;
  (void)hipFuncSetAttribute((const void*)k_proj, hipFuncAttributeMaxDynamicSharedMemorySize, L1);
  (void)hipFuncSetAttribute((const void*)k_attn, hipFuncAttributeMaxDynamicSharedMemorySize, L2);
  (void)hipFuncSetAttribute((const void*)k_out, hipFuncAttributeMaxDynamicSharedMemorySize, L3);
  k_proj<<<256, 512, L1, stream>>>(xa, wqkv, xb, bre, bim, lre, lim, logdt, ws);
  k_attn<<<256, 512, L2, stream>>>(ws);
  k_out<<<192, 512, L3, stream>>>(xb, cre, cim, dskip, wo, ws, (float*)d_out);
}
```

```cpp
#include <hip/hip_runtime.h>

typedef unsigned short u16;
typedef __attribute__((ext_vector_type(8))) __bf16 bf16x8;
typedef __attribute__((ext_vector_type(16))) __bf16 bf16x16;
typedef __attribute__((ext_vector_type(2))) __bf16 bf16x2;
typedef __attribute__((ext_vector_type(4))) short s16x4;
typedef __attribute__((ext_vector_type(2))) float f32x2;
typedef __attribute__((ext_vector_type(4))) float f32x4;
typedef __attribute__((ext_vector_type(8))) float f32x8;
typedef __attribute__((ext_vector_type(16))) float f32x16;
typedef __attribute__((ext_vector_type(32))) float f32x32;
typedef __attribute__((ext_vector_type(2))) unsigned u32x2;
typedef __attribute__((ext_vector_type(4))) unsigned u32x4;
typedef __attribute__((ext_vector_type(16))) unsigned u32x16;
typedef __attribute__((address_space(3))) char* lds_ptr;
typedef __amdgpu_buffer_rsrc_t rsrc_t;
#define LDS(T) __attribute__((address_space(3))) T*

#define DI __device__ __forceinline__
#define SBAR() __builtin_amdgcn_sched_barrier(0)
#define PIN(x) asm volatile("" : "+v"(x))
#define SPIN(x) asm volatile("" : "+s"(x))
#define FENCE() asm volatile("" ::: "memory")
#define WAIT_VM(n) asm volatile("s_waitcnt vmcnt(%0)" ::"n"(n) : "memory")
#define BAR() asm volatile("s_barrier" ::: "memory")
#define BARRIER() do { FENCE(); __builtin_amdgcn_s_waitcnt(0xC07F); BAR(); } while (0)

constexpr unsigned KV_REC = 2432, KV_HEAD = 64 * KV_REC;
constexpr unsigned Q_REC = 1280, Q_HEAD = 64 * Q_REC;
constexpr unsigned H_TILE = 36864, O_TILE = 36864;
constexpr unsigned WS_KV = 0;
constexpr unsigned WS_Q = WS_KV + 32 * KV_HEAD;
constexpr unsigned WS_H = WS_Q + 32 * Q_HEAD;
constexpr unsigned WS_O = WS_H + 128 * H_TILE;
constexpr unsigned WS_AG = WS_O + 64 * O_TILE;
constexpr unsigned WS_F1 = WS_AG + 18 * 16 * 256;
constexpr unsigned WS_F2 = WS_F1 + 8192;
constexpr unsigned WS_F3 = WS_F2 + 32768;
constexpr unsigned WS_F4 = WS_F3 + 64 * 18 * 128;
constexpr unsigned WS_END = WS_F4 + 131072;

constexpr unsigned L_SLOT = 32768, L_W = 16384, L_SPARE = KV_HEAD, L_BAD = L_SPARE + 7168, L_SINK = L_BAD + 256, L_ALL = 163840;
constexpr unsigned L_C = 82944, L_CS = 18432, L_XCH = L_C + 2 * L_CS, L_XCH3 = L_C + 4 * L_CS;
constexpr unsigned L_PAR = L_SLOT + 24576;
constexpr unsigned L_SROW = 288, L_ST = 2 * L_CS, L_STY = L_C + L_CS, L_STO = L_C;

constexpr int SC1 = 16;
constexpr unsigned EMPTY = 0xffffffffu;
constexpr int SPIN_MAX = 1 << 16;
constexpr int SA = 2, SB = 1;
constexpr int TWO_SLEEP = 8, JS9 = 2, H_SLEEP = 8, V_TOUCH = 2;
constexpr unsigned H_LIMIT = 9800;
constexpr int PUB_AT = 1;

DI unsigned pack_bf16(float lo, float hi) {
  f32x2 v = {lo, hi};
  return __builtin_bit_cast(unsigned, __builtin_convertvector(v, bf16x2));
}
DI float bf_lo(unsigned x) { return __uint_as_float(x << 16); }
DI float bf_hi(unsigned x) { return __uint_as_float(x & 0xffff0000u); }
DI float bf_val(u16 x) { return __uint_as_float((unsigned)x << 16); }
DI u32x4 pack8(const f32x16& a, int o, float s) {
  return u32x4{pack_bf16(a[o] * s, a[o + 1] * s), pack_bf16(a[o + 2] * s, a[o + 3] * s),
               pack_bf16(a[o + 4] * s, a[o + 5] * s), pack_bf16(a[o + 6] * s, a[o + 7] * s)};
}
DI u32x4 pack8(const f32x16& a, int o) {
  return u32x4{pack_bf16(a[o], a[o + 1]), pack_bf16(a[o + 2], a[o + 3]), pack_bf16(a[o + 4], a[o + 5]), pack_bf16(a[o + 6], a[o + 7])};
}

#define GL "global_load_lds_dwordx4 "
#define DMA_FNS(NAME, POL)                                                                                      \
  DI void NAME##1(const void* base, unsigned v0, unsigned dst) {                                                \
    asm volatile("s_mov_b32 m0, %2\n s_nop 0\n " GL "%0, %1" POL ::"v"(v0), "s"(base), "s"(dst) : "memory");    \
  }                                                                                                             \
  DI void NAME##2(const void* base, unsigned v0, unsigned v1, unsigned dst) {                                   \
    asm volatile("s_mov_b32 m0, %3\n s_nop 0\n " GL "%0, %2" POL "\n " GL "%1, %2 offset:1024" POL              \
                 ::"v"(v0), "v"(v1), "s"(base), "s"(dst) : "memory");                                           \
  }                                                                                                             \
  DI void NAME##4(const void* base, unsigned v0, unsigned v1, unsigned v2, unsigned v3, unsigned dst) {         \
    asm volatile("s_mov_b32 m0, %5\n s_nop 0\n " GL "%0, %4" POL "\n " GL "%1, %4 offset:1024" POL "\n "        \
                 GL "%2, %4 offset:2048" POL "\n " GL "%3, %4 offset:3072" POL                                  \
                 ::"v"(v0), "v"(v1), "v"(v2), "v"(v3), "s"(base), "s"(dst) : "memory");                         \
  }
DMA_FNS(dma, "")
DMA_FNS(dev_dma, " sc1")
template <int N>
DI void sync_dma2(const void* base, unsigned v0, unsigned v1, unsigned dst) {
  asm volatile("s_mov_b32 m0, %3\n s_waitcnt vmcnt(%4)\n s_barrier\n " GL "%0, %2\n " GL "%1, %2 offset:1024"
               ::"v"(v0), "v"(v1), "s"(base), "s"(dst), "n"(N) : "memory");
}
template <int N>
DI void sync_dma4(const void* base, unsigned v0, unsigned v1, unsigned v2, unsigned v3, unsigned dst) {
  asm volatile("s_mov_b32 m0, %5\n s_waitcnt vmcnt(%6)\n s_barrier\n " GL "%0, %4\n " GL "%1, %4 offset:1024\n "
               GL "%2, %4 offset:2048\n " GL "%3, %4 offset:3072"
               ::"v"(v0), "v"(v1), "v"(v2), "v"(v3), "s"(base), "s"(dst), "n"(N) : "memory");
}
template <int N>
DI void dma_c(const void* base, const unsigned (&v)[5], unsigned long long half, unsigned dst, unsigned dst2) {
  asm volatile("s_mov_b32 m0, %7\n .if %9 >= 0\n s_waitcnt vmcnt(%9)\n s_barrier\n .else\n s_nop 0\n .endif\n "
               GL "%0, %5\n " GL "%1, %5 offset:1024\n " GL "%2, %5 offset:2048\n " GL "%3, %5 offset:3072\n "
               "s_mov_b32 m0, %8\n s_mov_b64 exec, %6\n " GL "%4, %5\n s_mov_b64 exec, -1"
               ::"v"(v[0]), "v"(v[1]), "v"(v[2]), "v"(v[3]), "v"(v[4]), "s"(base), "s"(half), "s"(dst), "s"(dst2), "n"(N) : "memory");
}
DI void dma4_rows(const void* base, const unsigned (&v)[4], unsigned dst) {
  asm volatile("s_mov_b32 m0, %5\n s_nop 0\n " GL "%0, %4\n " GL "%1, %4 offset:576\n " GL "%2, %4 offset:1152\n " GL "%3, %4 offset:1728"
               ::"v"(v[0]), "v"(v[1]), "v"(v[2]), "v"(v[3]), "s"(base), "s"(dst) : "memory");
}
DI void touch(const void* base, unsigned v) {
  asm volatile("s_mov_b32 m0, %2\n s_nop 0\n global_load_lds_dword %0, %1 sc1" ::"v"(v), "s"(base), "s"(L_SINK) : "memory");
}
#define RAW_LD(op, d, v, base) asm volatile(op : "=v"(d) : "v"(v), "s"(base) : "memory")

DI bf16x8 lds_frag(unsigned a) { return *(LDS(const bf16x8))(size_t)a; }
DI bf16x8 lds_frag(lds_ptr p) { return *(LDS(const bf16x8))p; }
DI bf16x8 lds_frag_tr(lds_ptr a, lds_ptr b) {
  const s16x4 lo = __builtin_amdgcn_ds_read_tr16_b64_v4i16((LDS(s16x4))a);
  const s16x4 hi = __builtin_amdgcn_ds_read_tr16_b64_v4i16((LDS(s16x4))b);
  return __builtin_bit_cast(bf16x8, __builtin_shufflevector(lo, hi, 0, 1, 2, 3, 4, 5, 6, 7));
}
#define MFMA32(a, b, c) __builtin_amdgcn_mfma_f32_32x32x16_bf16((a), (b), (c), 0, 0, 0)
DI void mma16(f32x4& d, bf16x8 a, u32x4 b, bool first) {
  if (first) asm volatile("v_mfma_f32_16x16x32_bf16 %0, %1, %2, 0" : "=&v"(d) : "v"(a), "v"(b));
  else asm volatile("v_mfma_f32_16x16x32_bf16 %0, %1, %2, %0" : "+v"(d) : "v"(a), "v"(b));
}
#define LD32(o, s) __builtin_amdgcn_raw_buffer_load_b32(rs, (o), (s), SC1)
#define LD64(o, s) __builtin_amdgcn_raw_buffer_load_b64(rs, (o), (s), SC1)
#define LD128(o, s) __builtin_amdgcn_raw_buffer_load_b128(rs, (o), (s), SC1)
#define ST32(v, o, s) __builtin_amdgcn_raw_buffer_store_b32((v), rs, (o), (s), SC1)
#define ST64(v, o, s) __builtin_amdgcn_raw_buffer_store_b64((v), rs, (o), (s), SC1)
#define ST128(v, o) __builtin_amdgcn_raw_buffer_store_b128((v), rs, (o), 0, SC1)
template <int N> struct Int { static constexpr int value = N; };
template <int I, int N, class F>
DI void expand(F&& f) {
  if constexpr (I < N) { f(Int<I>{}); expand<I + 1, N>(f); }
}
DI bool all_lanes(bool p) { return __builtin_amdgcn_ballot_w64(!p) == 0; }


constexpr unsigned slot_of(int c) { return (c + 2) % 5 * L_SLOT; }
constexpr unsigned slot_base(int c) { return slot_of(c) & ~0xffffu; }
constexpr unsigned slot_imm(int c) { return slot_of(c) & 0xffffu; }
template <int N>
DI void spread() {
#pragma unroll
  for (int i = 0; i < N; ++i) {
    __builtin_amdgcn_sched_group_barrier(0x8, 1, 0);
    __builtin_amdgcn_sched_group_barrier(0x6, 5, 0);
  }
  SBAR();
}

struct BuTile {
  unsigned xa[4], ba[4];
  bf16x8 f[2][12];
  DI void init(int w, int lane) {
    const int r = lane & 31, h = lane >> 5, tok = 16 * ((r >> 2) & 1) + (r & 3) + 4 * (r >> 3);
#pragma unroll
    for (int ks = 0; ks < 4; ++ks) {
      xa[ks] = ((32 * w + tok) * 128 + ((h ^ ((tok >> 1) & 7)) << 4)) ^ (ks << 5);
      ba[ks] = (L_W + r * 128 + ((h ^ ((r >> 1) & 7)) << 4)) ^ (ks << 5);
      PIN(xa[ks]); PIN(ba[ks]);
    }
  }
  DI void move(unsigned d, int k0 = 0, int k1 = 4) {
#pragma unroll
    for (int ks = k0; ks < k1; ++ks) { xa[ks] += d; ba[ks] += d; PIN(xa[ks]); PIN(ba[ks]); }
  }
  DI void load(int s, int ks, unsigned imm) {
    f[s][3 * ks] = lds_frag((lds_ptr)(size_t)xa[ks] + imm);
    f[s][3 * ks + 1] = lds_frag((lds_ptr)(size_t)ba[ks] + imm);
    f[s][3 * ks + 2] = lds_frag((lds_ptr)(size_t)ba[ks] + imm + 4096);
  }
  DI void mma(int s, int i, f32x16& re, f32x16& im) {
    if (i & 1) im = MFMA32(f[s][3 * (i >> 1)], f[s][3 * (i >> 1) + 2], im);
    else re = MFMA32(f[s][3 * (i >> 1)], f[s][3 * (i >> 1) + 1], re);
  }
};

template <bool VMODE, class Hook, class Fill>
DI void gemm_qkv(unsigned (&xa)[4], unsigned (&wa)[2], f32x16 (&acc)[2][2], Hook hook, Fill fill) {
  bf16x8 xf[4][2], wf[4][2];
  auto load = [&](int c, int n) {
    const int ks = n >> 1, i = n & 1;
    xf[ks][i] = lds_frag((lds_ptr)(size_t)xa[ks] + slot_imm(c) + i * 4096);
    const lds_ptr p = (lds_ptr)(size_t)wa[i] + slot_imm(c) + ks * 4096;
    wf[ks][i] = lds_frag_tr(p, p + 1024);
    SBAR();
  };
  auto mma = [&](int ks, int i, int j) {
    acc[i][j] = VMODE ? MFMA32(xf[ks][i], wf[ks][j], acc[i][j]) : MFMA32(wf[ks][j], xf[ks][i], acc[i][j]);
  };
  BAR();
#pragma unroll
  for (int n = 0; n < 4; ++n) load(0, n);
#pragma unroll
  for (int c = 0; c < 9; ++c) {
#pragma unroll
    for (int m = 0; m < 4; ++m) { mma(0, m & 1, m >> 1); SBAR(); load(c, 4 + m); }
#pragma unroll
    for (int m = 0; m < 3; ++m) mma(1, m & 1, m >> 1);
    if (c < 8 && slot_base(c + 1) != slot_base(c)) {
#pragma unroll
      for (int i = 0; i < 4; ++i) { xa[i] += slot_base(c + 1) - slot_base(c); PIN(xa[i]); }
#pragma unroll
      for (int i = 0; i < 2; ++i) { wa[i] += slot_base(c + 1) - slot_base(c); PIN(wa[i]); }
    }
    hook(c);
    spread<3>();
    mma(1, 1, 1);
    SBAR();
    if (c < 8) BARRIER();
    SBAR();
#pragma unroll
    for (int m = 0; m < 4; ++m) {
      if (c < 8) { mma(2, m & 1, m >> 1); SBAR(); load(c + 1, m); }
      else { mma(2 + (m >> 1), m & 1, 0); SBAR(); }
    }
#pragma unroll
    for (int m = 0; m < 4; ++m) {
      if (c < 8) mma(3, m & 1, m >> 1);
      else mma(2 + (m >> 1), m & 1, 1);
    }
    fill(c);
    spread<4>();
  }
}

DI f32x2 cfma(f32x2 a, f32x2 x, f32x2 c) {
  f32x2 d;
  asm("v_pk_fma_f32 %0, %1, %2, %3 op_sel:[1,1,0] op_sel_hi:[1,0,1] neg_lo:[1,0,0]\n"
      "v_pk_fma_f32 %0, %1, %2, %0 op_sel_hi:[0,1,1]" : "=&v"(d) : "v"(a), "v"(x), "v"(c));
  return d;
}
DI f32x2 cmul(f32x2 a, f32x2 x) {
  f32x2 d;
  asm("v_pk_mul_f32 %0, %1, %2 op_sel:[1,1] op_sel_hi:[1,0] neg_lo:[1,0]\n"
      "v_pk_fma_f32 %0, %1, %2, %0 op_sel_hi:[0,1,1]" : "=&v"(d) : "v"(a), "v"(x));
  return d;
}
DI f32x2 csq(f32x2 a) { return cmul(a, a); }

struct Scan {
  f32x2 u[16];
  f32x2 a, k, a16, a128, a1024, aseg, e, intra;
  u32x2 ag[8];
};

DI void scan_params(Scan& s, unsigned p, int h) {
  const f32x4 x = *(LDS(const f32x4))(size_t)p, y = *(LDS(const f32x4))((lds_ptr)(size_t)p + 1024);
  s.a = f32x2{x[0], x[1]}; s.k = f32x2{x[2], x[3]};
  s.a16 = f32x2{y[0], y[1]}; s.a128 = f32x2{y[2], y[3]};
  s.aseg = *(LDS(const f32x2))((lds_ptr)(size_t)(p + 8 * h) + 2048);
  s.a1024 = *(LDS(const f32x2))((lds_ptr)(size_t)p + 3072);
}
DI void scan_local(Scan& s, f32x16 re, f32x16 im, unsigned row) {
  asm volatile("s_nop 11" : "+v"(re), "+v"(im));
#pragma unroll
  for (int i = 0; i < 16; i += 2) {
    asm("v_pk_mul_f32 %0, %2, %4 op_sel_hi:[0,1] neg_hi:[0,1]\n"
        "v_pk_mul_f32 %1, %2, %4 op_sel:[1,0] neg_hi:[0,1]\n"
        "v_pk_fma_f32 %0, %3, %4, %0 op_sel:[0,1,0] op_sel_hi:[0,0,1] neg_lo:[0,1,0] neg_hi:[0,1,0]\n"
        "v_pk_fma_f32 %1, %3, %4, %1 op_sel:[1,1,0] op_sel_hi:[1,0,1] neg_lo:[0,1,0] neg_hi:[0,1,0]"
        : "=&v"(s.u[i]), "=&v"(s.u[i + 1]) : "v"(f32x2{re[i], re[i + 1]}), "v"(f32x2{im[i], im[i + 1]}), "v"(s.k));
    s.e = cfma(s.a, i ? cfma(s.a, s.e, s.u[i]) : s.u[0], s.u[i + 1]);
  }
  *(LDS(f32x2))(size_t)row = s.e;
}
template <int STEP>
DI void scan_tile(Scan& s, rsrc_t rs, unsigned row, unsigned ag_vo) {
  const lds_ptr p = (lds_ptr)(size_t)(STEP > 0 ? row - 7 * 256 : row);
  f32x2 c = *(LDS(const f32x2))(p + (STEP > 0 ? 0 : 7 * 256));
#pragma unroll
  for (int i = 1; i < 7; ++i) c = cfma(s.a16, c, *(LDS(const f32x2))(p + (STEP > 0 ? i : 7 - i) * 256));
  s.intra = c;
  c = cfma(s.a16, c, s.e);
  ST64((u32x2{min(__float_as_uint(c[0]), EMPTY - 1), min(__float_as_uint(c[1]), EMPTY - 1)}), ag_vo, 0);
}
DI void scan_fetch(Scan& s, rsrc_t rs, unsigned& vo) {
  PIN(vo);
#pragma unroll
  for (int i = 0; i < 8; ++i) s.ag[i] = LD64(vo + i * 256, 0);
}
DI void scan_finish(Scan& s, rsrc_t rs, rsrc_t ag, unsigned& ag_vo, unsigned t_w, unsigned t_r, unsigned h_vo) {
#pragma unroll 1
  for (int spin = 0; spin < SPIN_MAX; ++spin) {
    unsigned m = 0;
#pragma unroll
    for (int i = 0; i < 8; ++i) m = max(m, max(s.ag[i][0], s.ag[i][1]));
    if (all_lanes(m != EMPTY)) break;
    __builtin_amdgcn_s_sleep(8);
    FENCE();
    scan_fetch(s, ag, ag_vo);
  }
  f32x2 c = __builtin_bit_cast(f32x2, s.ag[7]);
#pragma unroll
  for (int i = 6; i >= 0; --i) c = cfma(s.a128, c, __builtin_bit_cast(f32x2, s.ag[i]));
  const u32x2 x = __builtin_amdgcn_permlane32_swap(__float_as_uint(c[0]), __float_as_uint(c[0]), false, false);
  const u32x2 y = __builtin_amdgcn_permlane32_swap(__float_as_uint(c[1]), __float_as_uint(c[1]), false, false);
  c = cfma(s.a1024, f32x2{__uint_as_float(x[1]), __uint_as_float(y[1])}, f32x2{__uint_as_float(x[0]), __uint_as_float(y[0])});
  c = cfma(s.aseg, c, s.intra);
#pragma unroll
  for (int i = 0; i < 16; ++i) {
    c = cfma(s.a, c, s.u[i]);
    s.u[i] = c;
  }
#pragma unroll
  for (int i = 0; i < 4; ++i) {
    const int t = 8 * (i & 1), p = i >> 1;
    *(LDS(u32x4))(size_t)t_w = u32x4{pack_bf16(s.u[t][p], s.u[t + 1][p]), pack_bf16(s.u[t + 2][p], s.u[t + 3][p]),
                                     pack_bf16(s.u[t + 4][p], s.u[t + 5][p]), pack_bf16(s.u[t + 6][p], s.u[t + 7][p])};
    FENCE();
    const bf16x8 f = lds_frag_tr((lds_ptr)(size_t)t_r, (lds_ptr)(size_t)(t_r ^ 64));
    FENCE();
    ST128(__builtin_bit_cast(u32x4, f), h_vo + p * 18432 + t * 16);
  }
}

#define ATTN_LOOP \
  "ds_read_b64 v[148:149], v177\n ds_read_b128 v[150:153], v175\n s_mov_b64 exec, %[vm]\n ds_read_b128 v[158:161], v176 offset:0\n " \
  "ds_read_b128 v[162:165], v176 offset:576\n s_mov_b64 exec, -1\n s_mov_b32 %[it], 0\n s_waitcnt lgkmcnt(3)\n " \
  "v_mfma_f32_32x32x4_2b_bf16 v[0:31], v[148:149], v[144:145], 0 cbsz:1 abid:0\n s_nop 15\n s_nop 0\n s_waitcnt lgkmcnt(0)\n " \
  "v_mfma_f32_32x32x16_bf16 v[0:15], v[150:153], v[128:131], v[0:15]\n " \
  "v_mfma_f32_32x32x16_bf16 v[16:31], v[150:153], v[132:135], v[16:31]\n s_nop 10\n Lloop_%=:\n v_exp_f32 v0, v0\n v_exp_f32 v1, v1\n " \
  "v_smfmac_f32_32x32x32_bf16 v[112:127], v[170:173], v[56:63], v174\n s_mov_b64 exec, %[vm]\n " \
  "ds_read_b128 v[166:169], v176 offset:4864\n ds_read_b128 v[170:173], v176 offset:5440\n s_mov_b64 exec, -1\n v_exp_f32 v2, v2\n " \
  "v_exp_f32 v3, v3\n v_mfma_f32_32x32x4_2b_bf16 v[32:63], v[148:149], v[146:147], 0 cbsz:1 abid:0\n " \
  "ds_read_b128 v[154:157], v175 offset:4864\n v_exp_f32 v4, v4\n v_exp_f32 v5, v5\n v_exp_f32 v6, v6\n v_exp_f32 v7, v7\n " \
  "v_exp_f32 v8, v8\n v_exp_f32 v9, v9\n v_exp_f32 v10, v10\n v_exp_f32 v11, v11\n " \
  "v_smfmac_f32_32x32x32_bf16 v[64:79], v[158:161], v[0:7], v174\n v_exp_f32 v12, v12\n v_exp_f32 v13, v13\n v_exp_f32 v14, v14\n " \
  "v_exp_f32 v15, v15\n v_exp_f32 v16, v16\n v_exp_f32 v17, v17\n v_smfmac_f32_32x32x32_bf16 v[64:79], v[162:165], v[8:15], v174\n " \
  "v_exp_f32 v18, v18\n v_exp_f32 v19, v19\n v_exp_f32 v20, v20\n v_mfma_f32_32x32x16_bf16 v[32:47], v[150:153], v[136:139], v[32:47]\n " \
  "v_exp_f32 v21, v21\n v_exp_f32 v22, v22\n v_exp_f32 v23, v23\n v_exp_f32 v24, v24\n v_exp_f32 v25, v25\n " \
  "v_smfmac_f32_32x32x32_bf16 v[80:95], v[158:161], v[16:23], v174\n v_exp_f32 v26, v26\n v_exp_f32 v27, v27\n v_exp_f32 v28, v28\n " \
  "v_mfma_f32_32x32x16_bf16 v[48:63], v[150:153], v[140:143], v[48:63]\n v_exp_f32 v29, v29\n v_exp_f32 v30, v30\n v_exp_f32 v31, v31\n " \
  "v_exp_f32 v32, v32\n v_exp_f32 v33, v33\n v_smfmac_f32_32x32x32_bf16 v[80:95], v[162:165], v[24:31], v174\n s_waitcnt lgkmcnt(0)\n " \
  "v_exp_f32 v34, v34\n v_exp_f32 v35, v35\n v_mfma_f32_32x32x4_2b_bf16 v[0:31], v[148:149], v[144:145], 0 cbsz:1 abid:1\n " \
  "v_exp_f32 v36, v36\n v_exp_f32 v37, v37\n v_exp_f32 v38, v38\n v_exp_f32 v39, v39\n v_exp_f32 v40, v40\n v_exp_f32 v41, v41\n " \
  "v_exp_f32 v42, v42\n v_exp_f32 v43, v43\n v_smfmac_f32_32x32x32_bf16 v[96:111], v[158:161], v[32:39], v174\n v_exp_f32 v44, v44\n " \
  "v_exp_f32 v45, v45\n v_exp_f32 v46, v46\n v_exp_f32 v47, v47\n v_exp_f32 v48, v48\n v_exp_f32 v49, v49\n " \
  "v_smfmac_f32_32x32x32_bf16 v[96:111], v[162:165], v[40:47], v174\n v_exp_f32 v50, v50\n v_exp_f32 v51, v51\n v_exp_f32 v52, v52\n " \
  "v_mfma_f32_32x32x16_bf16 v[0:15], v[154:157], v[128:131], v[0:15]\n v_exp_f32 v53, v53\n v_exp_f32 v54, v54\n v_exp_f32 v55, v55\n " \
  "v_exp_f32 v56, v56\n v_exp_f32 v57, v57\n v_smfmac_f32_32x32x32_bf16 v[112:127], v[158:161], v[48:55], v174\n v_exp_f32 v58, v58\n " \
  "v_exp_f32 v59, v59\n v_exp_f32 v60, v60\n v_mfma_f32_32x32x16_bf16 v[16:31], v[154:157], v[132:135], v[16:31]\n v_exp_f32 v61, v61\n " \
  "v_exp_f32 v62, v62\n v_exp_f32 v63, v63\n v_exp_f32 v0, v0\n v_exp_f32 v1, v1\n " \
  "v_smfmac_f32_32x32x32_bf16 v[112:127], v[162:165], v[56:63], v174\n s_mov_b64 exec, %[vm]\n " \
  "ds_read_b128 v[158:161], v176 offset:9728\n ds_read_b128 v[162:165], v176 offset:10304\n s_mov_b64 exec, -1\n v_exp_f32 v2, v2\n " \
  "v_exp_f32 v3, v3\n v_mfma_f32_32x32x4_2b_bf16 v[32:63], v[148:149], v[146:147], 0 cbsz:1 abid:1\n " \
  "ds_read_b128 v[150:153], v175 offset:9728\n ds_read_b64 v[148:149], v177 offset:9728\n v_exp_f32 v4, v4\n v_exp_f32 v5, v5\n " \
  "v_exp_f32 v6, v6\n v_exp_f32 v7, v7\n v_exp_f32 v8, v8\n v_exp_f32 v9, v9\n v_exp_f32 v10, v10\n v_exp_f32 v11, v11\n " \
  "v_smfmac_f32_32x32x32_bf16 v[64:79], v[166:169], v[0:7], v174\n v_exp_f32 v12, v12\n v_exp_f32 v13, v13\n v_exp_f32 v14, v14\n " \
  "v_exp_f32 v15, v15\n v_exp_f32 v16, v16\n v_exp_f32 v17, v17\n v_smfmac_f32_32x32x32_bf16 v[64:79], v[170:173], v[8:15], v174\n " \
  "v_exp_f32 v18, v18\n v_exp_f32 v19, v19\n v_exp_f32 v20, v20\n v_mfma_f32_32x32x16_bf16 v[32:47], v[154:157], v[136:139], v[32:47]\n " \
  "v_exp_f32 v21, v21\n v_exp_f32 v22, v22\n v_exp_f32 v23, v23\n v_exp_f32 v24, v24\n v_exp_f32 v25, v25\n " \
  "v_smfmac_f32_32x32x32_bf16 v[80:95], v[166:169], v[16:23], v174\n v_exp_f32 v26, v26\n v_exp_f32 v27, v27\n v_exp_f32 v28, v28\n " \
  "v_mfma_f32_32x32x16_bf16 v[48:63], v[154:157], v[140:143], v[48:63]\n v_exp_f32 v29, v29\n v_exp_f32 v30, v30\n v_exp_f32 v31, v31\n " \
  "v_exp_f32 v32, v32\n v_exp_f32 v33, v33\n v_smfmac_f32_32x32x32_bf16 v[80:95], v[170:173], v[24:31], v174\n s_waitcnt lgkmcnt(0)\n " \
  "v_exp_f32 v34, v34\n v_exp_f32 v35, v35\n v_mfma_f32_32x32x4_2b_bf16 v[0:31], v[148:149], v[144:145], 0 cbsz:1 abid:0\n " \
  "v_exp_f32 v36, v36\n v_exp_f32 v37, v37\n v_exp_f32 v38, v38\n v_exp_f32 v39, v39\n v_exp_f32 v40, v40\n v_exp_f32 v41, v41\n " \
  "v_exp_f32 v42, v42\n v_exp_f32 v43, v43\n v_smfmac_f32_32x32x32_bf16 v[96:111], v[166:169], v[32:39], v174\n v_exp_f32 v44, v44\n " \
  "v_exp_f32 v45, v45\n v_exp_f32 v46, v46\n v_exp_f32 v47, v47\n v_exp_f32 v48, v48\n v_exp_f32 v49, v49\n " \
  "v_smfmac_f32_32x32x32_bf16 v[96:111], v[170:173], v[40:47], v174\n v_exp_f32 v50, v50\n v_exp_f32 v51, v51\n v_exp_f32 v52, v52\n " \
  "v_mfma_f32_32x32x16_bf16 v[0:15], v[150:153], v[128:131], v[0:15]\n v_exp_f32 v53, v53\n v_exp_f32 v54, v54\n v_exp_f32 v55, v55\n " \
  "v_exp_f32 v56, v56\n v_exp_f32 v57, v57\n v_smfmac_f32_32x32x32_bf16 v[112:127], v[166:169], v[48:55], v174\n v_exp_f32 v58, v58\n " \
  "v_exp_f32 v59, v59\n v_exp_f32 v60, v60\n v_mfma_f32_32x32x16_bf16 v[16:31], v[150:153], v[132:135], v[16:31]\n v_exp_f32 v61, v61\n " \
  "v_exp_f32 v62, v62\n v_exp_f32 v63, v63\n v_exp_f32 v0, v0\n v_exp_f32 v1, v1\n " \
  "v_smfmac_f32_32x32x32_bf16 v[112:127], v[170:173], v[56:63], v174\n s_mov_b64 exec, %[vm]\n " \
  "ds_read_b128 v[166:169], v176 offset:14592\n ds_read_b128 v[170:173], v176 offset:15168\n s_mov_b64 exec, -1\n v_exp_f32 v2, v2\n " \
  "v_exp_f32 v3, v3\n v_mfma_f32_32x32x4_2b_bf16 v[32:63], v[148:149], v[146:147], 0 cbsz:1 abid:0\n " \
  "ds_read_b128 v[154:157], v175 offset:14592\n v_exp_f32 v4, v4\n v_exp_f32 v5, v5\n v_exp_f32 v6, v6\n v_exp_f32 v7, v7\n " \
  "v_exp_f32 v8, v8\n v_exp_f32 v9, v9\n v_exp_f32 v10, v10\n v_exp_f32 v11, v11\n " \
  "v_smfmac_f32_32x32x32_bf16 v[64:79], v[158:161], v[0:7], v174\n v_exp_f32 v12, v12\n v_exp_f32 v13, v13\n v_exp_f32 v14, v14\n " \
  "v_exp_f32 v15, v15\n v_exp_f32 v16, v16\n v_exp_f32 v17, v17\n v_smfmac_f32_32x32x32_bf16 v[64:79], v[162:165], v[8:15], v174\n " \
  "v_exp_f32 v18, v18\n v_exp_f32 v19, v19\n v_exp_f32 v20, v20\n v_mfma_f32_32x32x16_bf16 v[32:47], v[150:153], v[136:139], v[32:47]\n " \
  "v_exp_f32 v21, v21\n v_exp_f32 v22, v22\n v_exp_f32 v23, v23\n v_exp_f32 v24, v24\n v_exp_f32 v25, v25\n " \
  "v_smfmac_f32_32x32x32_bf16 v[80:95], v[158:161], v[16:23], v174\n v_exp_f32 v26, v26\n v_exp_f32 v27, v27\n v_exp_f32 v28, v28\n " \
  "v_mfma_f32_32x32x16_bf16 v[48:63], v[150:153], v[140:143], v[48:63]\n v_exp_f32 v29, v29\n v_exp_f32 v30, v30\n v_exp_f32 v31, v31\n " \
  "v_exp_f32 v32, v32\n v_exp_f32 v33, v33\n v_smfmac_f32_32x32x32_bf16 v[80:95], v[162:165], v[24:31], v174\n s_waitcnt lgkmcnt(0)\n " \
  "v_exp_f32 v34, v34\n v_exp_f32 v35, v35\n v_mfma_f32_32x32x4_2b_bf16 v[0:31], v[148:149], v[144:145], 0 cbsz:1 abid:1\n " \
  "v_exp_f32 v36, v36\n v_exp_f32 v37, v37\n v_exp_f32 v38, v38\n v_exp_f32 v39, v39\n v_exp_f32 v40, v40\n v_exp_f32 v41, v41\n " \
  "v_exp_f32 v42, v42\n v_exp_f32 v43, v43\n v_smfmac_f32_32x32x32_bf16 v[96:111], v[158:161], v[32:39], v174\n v_exp_f32 v44, v44\n " \
  "v_exp_f32 v45, v45\n v_exp_f32 v46, v46\n v_exp_f32 v47, v47\n v_exp_f32 v48, v48\n v_exp_f32 v49, v49\n " \
  "v_smfmac_f32_32x32x32_bf16 v[96:111], v[162:165], v[40:47], v174\n v_exp_f32 v50, v50\n v_exp_f32 v51, v51\n v_exp_f32 v52, v52\n " \
  "v_mfma_f32_32x32x16_bf16 v[0:15], v[154:157], v[128:131], v[0:15]\n v_exp_f32 v53, v53\n v_exp_f32 v54, v54\n v_exp_f32 v55, v55\n " \
  "v_exp_f32 v56, v56\n v_exp_f32 v57, v57\n v_smfmac_f32_32x32x32_bf16 v[112:127], v[158:161], v[48:55], v174\n v_exp_f32 v58, v58\n " \
  "v_exp_f32 v59, v59\n v_exp_f32 v60, v60\n v_mfma_f32_32x32x16_bf16 v[16:31], v[154:157], v[132:135], v[16:31]\n v_exp_f32 v61, v61\n " \
  "v_exp_f32 v62, v62\n v_exp_f32 v63, v63\n v_exp_f32 v0, v0\n v_exp_f32 v1, v1\n " \
  "v_smfmac_f32_32x32x32_bf16 v[112:127], v[162:165], v[56:63], v174\n s_mov_b64 exec, %[vm]\n " \
  "ds_read_b128 v[158:161], v176 offset:19456\n ds_read_b128 v[162:165], v176 offset:20032\n s_mov_b64 exec, -1\n v_exp_f32 v2, v2\n " \
  "v_exp_f32 v3, v3\n v_mfma_f32_32x32x4_2b_bf16 v[32:63], v[148:149], v[146:147], 0 cbsz:1 abid:1\n " \
  "ds_read_b128 v[150:153], v175 offset:19456\n ds_read_b64 v[148:149], v177 offset:19456\n v_exp_f32 v4, v4\n v_exp_f32 v5, v5\n " \
  "v_exp_f32 v6, v6\n v_exp_f32 v7, v7\n v_exp_f32 v8, v8\n v_exp_f32 v9, v9\n v_exp_f32 v10, v10\n v_exp_f32 v11, v11\n " \
  "v_smfmac_f32_32x32x32_bf16 v[64:79], v[166:169], v[0:7], v174\n v_exp_f32 v12, v12\n v_exp_f32 v13, v13\n v_exp_f32 v14, v14\n " \
  "v_exp_f32 v15, v15\n v_exp_f32 v16, v16\n v_exp_f32 v17, v17\n v_smfmac_f32_32x32x32_bf16 v[64:79], v[170:173], v[8:15], v174\n " \
  "v_exp_f32 v18, v18\n v_exp_f32 v19, v19\n v_exp_f32 v20, v20\n v_mfma_f32_32x32x16_bf16 v[32:47], v[154:157], v[136:139], v[32:47]\n " \
  "v_exp_f32 v21, v21\n v_exp_f32 v22, v22\n v_exp_f32 v23, v23\n v_exp_f32 v24, v24\n v_exp_f32 v25, v25\n " \
  "v_smfmac_f32_32x32x32_bf16 v[80:95], v[166:169], v[16:23], v174\n v_exp_f32 v26, v26\n v_exp_f32 v27, v27\n v_exp_f32 v28, v28\n " \
  "v_mfma_f32_32x32x16_bf16 v[48:63], v[154:157], v[140:143], v[48:63]\n v_exp_f32 v29, v29\n v_exp_f32 v30, v30\n v_exp_f32 v31, v31\n " \
  "v_exp_f32 v32, v32\n v_exp_f32 v33, v33\n v_smfmac_f32_32x32x32_bf16 v[80:95], v[170:173], v[24:31], v174\n s_waitcnt lgkmcnt(0)\n " \
  "v_exp_f32 v34, v34\n v_exp_f32 v35, v35\n v_mfma_f32_32x32x4_2b_bf16 v[0:31], v[148:149], v[144:145], 0 cbsz:1 abid:0\n " \
  "v_exp_f32 v36, v36\n v_exp_f32 v37, v37\n v_exp_f32 v38, v38\n v_exp_f32 v39, v39\n v_exp_f32 v40, v40\n v_exp_f32 v41, v41\n " \
  "v_exp_f32 v42, v42\n v_exp_f32 v43, v43\n v_smfmac_f32_32x32x32_bf16 v[96:111], v[166:169], v[32:39], v174\n v_exp_f32 v44, v44\n " \
  "v_exp_f32 v45, v45\n v_exp_f32 v46, v46\n v_exp_f32 v47, v47\n v_exp_f32 v48, v48\n v_exp_f32 v49, v49\n " \
  "v_smfmac_f32_32x32x32_bf16 v[96:111], v[170:173], v[40:47], v174\n v_exp_f32 v50, v50\n v_exp_f32 v51, v51\n v_exp_f32 v52, v52\n " \
  "v_mfma_f32_32x32x16_bf16 v[0:15], v[150:153], v[128:131], v[0:15]\n v_exp_f32 v53, v53\n v_exp_f32 v54, v54\n v_exp_f32 v55, v55\n " \
  "v_exp_f32 v56, v56\n v_exp_f32 v57, v57\n v_smfmac_f32_32x32x32_bf16 v[112:127], v[166:169], v[48:55], v174\n v_exp_f32 v58, v58\n " \
  "v_exp_f32 v59, v59\n v_exp_f32 v60, v60\n v_mfma_f32_32x32x16_bf16 v[16:31], v[150:153], v[132:135], v[16:31]\n v_exp_f32 v61, v61\n " \
  "v_exp_f32 v62, v62\n v_exp_f32 v63, v63\n v_add_u32 v175, 19456, v175\n v_add_u32 v176, 19456, v176\n v_add_u32 v177, 19456, v177\n " \
  "s_bitcmp1_b32 %[bm], %[it]\n s_cbranch_scc0 Lnobar_%=\n s_barrier\n Lnobar_%=:\n s_add_u32 %[it], %[it], 1\n s_cmp_lg_u32 %[it], 8\n " \
  "s_cbranch_scc1 Lloop_%=\n v_smfmac_f32_32x32x32_bf16 v[112:127], v[170:173], v[56:63], v174\n s_nop 11\n "

DI f32x16 lo16(const f32x32& x) { return __builtin_shufflevector(x, x, 0, 1, 2, 3, 4, 5, 6, 7, 8, 9, 10, 11, 12, 13, 14, 15); }
DI f32x16 hi16(const f32x32& x) { return __builtin_shufflevector(x, x, 16, 17, 18, 19, 20, 21, 22, 23, 24, 25, 26, 27, 28, 29, 30, 31); }
DI bf16x16 p_lo(const f32x16& x) { return __builtin_bit_cast(bf16x16, __builtin_shufflevector(x, x, 0, 1, 2, 3, 4, 5, 6, 7)); }
DI bf16x16 p_hi(const f32x16& x) { return __builtin_bit_cast(bf16x16, __builtin_shufflevector(x, x, 8, 9, 10, 11, 12, 13, 14, 15)); }
#define PV(o, v, p) o = __builtin_amdgcn_smfmac_f32_32x32x32_bf16((v), (p), (o), 0xDDDD, 0, 0)

DI void attn_shifted(int lane, bf16x8 q0, bf16x8 q1, s16x4 qr, unsigned vfill, f32x16& o0, f32x16& o1) {
  const int r = lane & 31, h = lane >> 5;
  float m0 = -__builtin_inff(), m1 = -__builtin_inff();
  o0 = f32x16{}; o1 = f32x16{};
#pragma unroll 1
  for (int t = 0; t < 128; ++t) {
    const unsigned rec = (t & 63) * KV_REC;
    const bf16x8 kf = lds_frag(rec + lane * 16);
    const f32x32 s = __builtin_amdgcn_mfma_f32_32x32x4bf16_1k(*(LDS(const s16x4))(size_t)(rec + 2176 + r * 8), qr, f32x32{}, 0, 0, 0);
    f32x16 s0 = MFMA32(kf, q0, lo16(s)), s1 = MFMA32(kf, q1, hi16(s));
    if (t < 64) {
#pragma unroll
      for (int i = 0; i < 16; ++i) { m0 = fmaxf(m0, s0[i]); m1 = fmaxf(m1, s1[i]); }
      if (t == 63) { m0 = fmaxf(m0, __shfl_xor(m0, 32)); m1 = fmaxf(m1, __shfl_xor(m1, 32)); }
    } else {
#pragma unroll
      for (int i = 0; i < 16; ++i) { s0[i] = __builtin_amdgcn_exp2f(s0[i] - m0); s1[i] = __builtin_amdgcn_exp2f(s1[i] - m1); }
      u32x4 v0 = {vfill, vfill, vfill, vfill}, v1 = v0;
      if (r < 18) {
        v0 = *(LDS(const u32x4))(size_t)(rec + 1024 + (h * 18 + r) * 16);
        v1 = *(LDS(const u32x4))(size_t)(rec + 1600 + (h * 18 + r) * 16);
      }
      const bf16x8 a0 = __builtin_bit_cast(bf16x8, v0), a1 = __builtin_bit_cast(bf16x8, v1);
      PV(o0, a0, p_lo(s0)); PV(o0, a1, p_hi(s0));
      PV(o1, a0, p_lo(s1)); PV(o1, a1, p_hi(s1));
    }
  }
}

DI void reload_kv(const char* kv, int w, int lane) {
  WAIT_VM(0);
  BARRIER();
#pragma unroll 1
  for (int i = w; i < 152; i += 8) dev_dma1(kv + i * 1024, lane * 16, i * 1024);
  WAIT_VM(0);
  BARRIER();
}

DI f32x4 staged(unsigned ra, unsigned imm, int i) { return *(LDS(const f32x4))((lds_ptr)(size_t)ra + imm + i * 3 * L_SROW); }
DI void store_rows(const f32x4 (&v)[6], char* dst, unsigned vo) {
  const rsrc_t rs = __builtin_amdgcn_make_buffer_rsrc(dst, 0, 15 * 4608 + 288, 0x00020000);
#pragma unroll
  for (int i = 0; i < 6; ++i) ST128(__builtin_bit_cast(u32x4, v[i]), vo + i * 3 * 4608);
}

DI void stage_tile(const f32x4 (&acc)[2][5], unsigned wa, int i, int j) {
  *(LDS(f32x4))((lds_ptr)(size_t)wa + i * 16 * L_SROW + j * 64) = acc[i][j];
}
DI void stage_done(f32x4 (&acc)[2][5], unsigned wa4, LDS(volatile unsigned) mine, LDS(volatile unsigned) other) {
  u32x4 t;
  asm volatile("s_nop 7" : "+v"(acc[0][4]), "+v"(acc[1][4]));
#pragma unroll
  for (int i = 0; i < 4; ++i) t[i] = __builtin_amdgcn_permlane32_swap(__float_as_uint(acc[0][4][i]), __float_as_uint(acc[1][4][i]), false, false)[0];
  *(LDS(u32x4))(size_t)wa4 = t;
  FENCE();
  *mine = 1;
#pragma unroll 1
  for (int spin = 0; spin < SPIN_MAX && !__builtin_amdgcn_readfirstlane(*other); ++spin) {}
  FENCE();
}

constexpr DI unsigned c_slot(int g) { return ((g & 3) ^ ((g & 2) >> 1)) * L_CS; }

__global__ __launch_bounds__(512, 2) void k_fused(const char* __restrict__ xa, const char* __restrict__ wqkv,
                                                  const char* __restrict__ wo, const char* __restrict__ xb,
                                                  const char* __restrict__ lre, const char* __restrict__ lim,
                                                  const char* __restrict__ bre, const char* __restrict__ bim,
                                                  const char* __restrict__ cre, const char* __restrict__ cim,
                                                  const char* __restrict__ logdt, const char* __restrict__ dskip,
                                                  char* __restrict__ out, char* ws) {
  const int b = blockIdx.x, xcd = b & 7, sb = b >> 3;
  const int rt = 4 * (xcd >> 1) + (sb & 3), cj = (xcd & 1 ? 0xfdcba987u : 0xe6543210u) >> (sb & 28) & 15;
  const int head = 4 * xcd + (sb & 3), qb = sb >> 2;
  const int t3 = head, c3 = qb;
  const bool two = sb >= 28;
  const int lane = threadIdx.x & 63, w = __builtin_amdgcn_readfirstlane(threadIdx.x >> 6);
  const int r = lane & 31, h = lane >> 5;
  unsigned rows0 = (lane >> 3) * 1152 + (((lane & 7) ^ (lane >> 4)) << 4), rows1 = (rows0 ^ 64) + 8192;
  unsigned xo2 = rows0 + 16384, xo3 = rows1 + 16384;
  int rot1 = 0x3186420u >> 4 * ((xcd >> 1) + 2 * (xcd & 1) + (qb & 1)) & 15;
  unsigned w0 = (lane >> 4) * 3456 + (((lane & 15) ^ ((lane >> 4) << 2)) << 4), w1 = w0 + 12800, w2 = w0 + 25600, w3 = w0 + 38400;
  asm volatile("" : "+v"(w0), "+v"(w1), "+v"(w2), "+v"(w3));
  if (two) { w0 = rows0; w1 = rows1; w2 = xo2; w3 = xo3; }
  unsigned x_off = (rt * 128 + 32 * (w & 3)) * 1152, x_rot = x_off + rot1 * 128, xd = (w & 3) * 4096;
  int w_step = two ? 128 : 64 * 3456;
  unsigned w_off = min(cj * 256, 3200) + (w & 3) * 16 * 3456, b_off = (cj + (w & 2)) * 32 * 1152;
  asm volatile("" : "+s"(w_off), "+s"(b_off));
  w_off = (two ? b_off : w_off) + rot1 * w_step;
  asm volatile("" : "+v"(rows0), "+v"(rows1), "+v"(xo2), "+v"(xo3), "+v"(w0), "+v"(w1), "+v"(w2), "+v"(w3));
  asm volatile("" : "+s"(x_off), "+s"(x_rot), "+s"(w_off), "+s"(xd), "+s"(w_step));
  unsigned code_sink = ((sb * 512 + threadIdx.x) >> 5) * 128;
  {
    unsigned long long pc;
    asm volatile("s_getpc_b64 %1\n Lcode_begin:\n v_min_u32 %0, Lcode_end - Lcode_begin, %0\n global_load_dword %0, %0, %1"
                 : "+v"(code_sink), "=&s"(pc)::"memory");
  }
  SBAR();
  asm volatile("" ::"s"(xa), "s"(wqkv), "s"(wo), "s"(xb), "s"(lre), "s"(lim), "s"(bre), "s"(bim), "s"(cre), "s"(cim), "s"(logdt),
               "s"(dskip), "s"(out), "s"(ws));
  LDS(volatile unsigned) bad = (LDS(volatile unsigned))(size_t)L_BAD;
#define LATER_BASES()                                                                \
  asm volatile("" : "+s"(ws), "+s"(cre), "+s"(cim));                                  \
  const rsrc_t rs = __builtin_amdgcn_make_buffer_rsrc(ws, 0, WS_END, 0x00020000);    \
  const char* kv = ws + WS_KV + head * KV_HEAD;                                      \
  const char* c_src = (w & 2 ? cim : cre) + (c3 * 72 + 32 * (w & 1)) * 1152;         \
  const unsigned long long c_half = w & 1 ? ~0xffffffffull : 0xffffffffull
#define ISSUE_C(N, g) dma_c<N>(c_src + (g) * 128, cv, c_half, L_C + c_slot(g) + (w & 2) * 4608 + (w & 1) * 4096, \
                               L_C + c_slot(g) + (w & 2) * 4608 + 8192)

  if (__builtin_expect(w >= 4, 1)) {
    const int lw = w - 4;
    const unsigned bd = L_W + lw * 2048;
    auto k1 = [&](const char* p, int c, int step) { SPIN(rot1); return p + (c + rot1 < 9 ? c : c - 9) * step; };
    int rot2;
    auto k2 = [&](const char* p, int c) { SPIN(rot2); return p + (c + rot2 < 18 ? (c - 9) * 128 : (c - 18) * 128); };
    __builtin_amdgcn_s_setprio(3);
    const char* x1 = (two ? xb : xa) + x_rot;
    dma4(x1, rows0, rows1, xo2, xo3, slot_of(0) + xd);
    SBAR();
    asm volatile("" : "+s"(bre), "+s"(bim));
    const char* wp = (two ? (w & 1 ? bim : bre) : wqkv) + w_off;
    dma4(wp, w0, w1, w2, w3, slot_of(0) + L_W + xd);
    SBAR();
    dma4(k1(x1, 1, 128), rows0, rows1, xo2, xo3, slot_of(1) + xd);
    __builtin_amdgcn_s_setprio(0);
    SBAR();
    LATER_BASES();
    rot2 = SA * (sb & 3) + SB * qb;
    rot2 -= rot2 > 8 ? 9 : 0;
    const char* x2 = xb + x_off + rot2 * 128;
    const char* b2 = (lw < 2 ? bre : bim) + (cj * 32 + 16 * (lw & 1)) * 1152 + rot2 * 128;
    auto x_b = [&](int c) { dma4(k2(x2, c), rows0, rows1, xo2, xo3, slot_of(c) + xd); };
    auto w_b = [&](int c) { dma2(k2(b2, c), rows0, rows1, slot_of(c) + bd); };
    u32x4 f0 = {}, f1 = {};
    const char* f1p = ws + WS_F1;
    auto f1_load = [&](u32x4& f) {
      if (lw == 3) asm volatile("global_load_dwordx4 %0, %1, %2 sc1" : "+v"(f) : "v"(lane * 128), "s"(f1p) : "memory");
    };
    unsigned f_own, f1v;
    unsigned par[3], par_v = (cj + (two ? 2 * h : 0)) * 64 + r * 2, ds_v = (lane - 18 * (__umul24(lane, 57) >> 10)) * 8, cl_f, cl_x, cl_a;
    u32x2 ds;
    {
      const int i = lane >> 3 & 3;
      unsigned f3 = WS_F3 + ((4 * rt + i) * 18 + cj) * 128, f4 = WS_F4 + ((4 * qb + i) * 32 + head) * 128;
      unsigned fx = two ? f3 + 256 : WS_F2 + (rt * 16 + cj) * 128;
      PIN(f3); PIN(f4); PIN(fx);
      cl_f = (h ? f4 : f3) + (lane & 7) * 16;
      cl_x = lane >= (two ? 32 : 8) ? WS_END : fx + (lane & 7) * 16;
      cl_a = h && !two ? WS_END : h * 8192 + r * 8;
      PIN(par_v); PIN(ds_v); PIN(cl_f); PIN(cl_x); PIN(cl_a);
    }
    unsigned cv[5] = {rows0, rows1, xo2, xo3}, st_w, st_w4, st_rd, st_g, x_r[4], x_w[4], o_m, o_m2, o_t, f4_w, f4_r, i_m, i_t, wa0, wa1;
    unsigned f2_kv, f2_q, qt, vfill;
    u32x16 vv;
    f32x32 oa, ob;
    f32x8 sz;
    u32x4 ad;
    int p_lw = lw, p_head = head, p_qb = qb;
    float dt, lr, li, mag, id, sn, cs;
    f32x2 a16;
    auto piece = [&](int n) __attribute__((always_inline)) {
      int l = lane;
      PIN(l);
      SPIN(p_lw); SPIN(p_head); SPIN(p_qb);
      const int lw = p_lw, head = p_head, qb = p_qb, t3 = head, xcd = head >> 2;
      const int r = l & 31, h = l >> 5, g = l >> 4, r16 = l & 15;
      if (n == 0) {
        asm volatile(".set reg, 56\n .rept 20\n v_mov_b64 v[reg:reg+1], 0\n .set reg, reg+2\n .endr" : "={v[56:63]}"(sz), "={v[64:95]}"(oa));
      } else if (n == 1) {
        asm volatile(".set reg, 96\n .rept 16\n v_mov_b64 v[reg:reg+1], 0\n .set reg, reg+2\n .endr" : "={v[96:127]}"(ob));
      } else if (n == 2) {
        vfill = (r | 4) == 22 ? 0x3f803f80u : 0u;
        qt = h * Q_REC + r * 8 + 1024;
        PIN(qt);
        asm volatile(".set reg, 158\n .rept 16\n v_mov_b32 v[reg], %1\n .set reg, reg+1\n .endr" : "=&{v[158:173]}"(vv) : "v"(vfill));
      } else if (n == 3 || n == 15) {
        {
          const int i = n == 15, t = lw ^ i;
          unsigned* x = i ? x_w : x_r;
          x[0] = L_XCH + (t >> 1) * 9216 + (t & 1) * 4096 + l * 16;
          x[1] = (t == 3 ? L_XCH3 + 4096 : L_XCH + (t >> 1) * 9216 + (t & 1) * 4096 + 3072) + l * 16;
          x[2] = L_XCH3 + t * 1024 + l * 16;
          x[3] = (t < 2 ? L_XCH + 8192 : L_XCH3 + 5120) + (t & 1) * 512 + l * 8;
          PIN(x[0]); PIN(x[1]); PIN(x[2]); PIN(x[3]);
        }
      } else if (n == 4) {
        const int row3 = __umul24(l, 57) >> 10;
        st_w = L_STO + (lw >> 1) * L_ST + (32 * (lw & 1) + r16) * L_SROW + g * 16;
        st_w4 = st_w + 256 + h * (16 * L_SROW - 32);
        st_rd = L_STO + (32 * (lw & 1) + 16 * (lw >> 1)) * L_SROW + l * 16;
        st_g = l < 54 ? row3 * (4608 - 288) + l * 16 : 0x100000;
        PIN(st_w); PIN(st_w4); PIN(st_rd); PIN(st_g);
      } else if (n == 5) {
        o_m = WS_O + (8 * qb + 2 * lw) * O_TILE + head * 1024 + l * 16; o_m2 = o_m + O_TILE; o_t = h * O_TILE + r * 4;
        f4_w = l == 0 ? WS_F4 + ((4 * qb + lw) * 32 + head) * 128 : WS_END; f4_r = WS_F4 + t3 * 4096 + r * 128;
        cv[4] = rows0 + (64 - 32 * (lw & 1)) * 1152;
        PIN(o_m); PIN(o_m2); PIN(o_t); PIN(f4_w); PIN(f4_r); PIN(cv[4]);
      } else if (n == 6) {
        i_m = g * 512 + r16 * 16; i_t = g * 512 + r16 * 4;
        wa1 = (l >> 2) * 288 + (l & 3) * 8; wa0 = wa1 + (lw >> 1) * 8 * 4608;
        wa1 += (16 + (lw >> 1)) * 4608;
        const int kh = lw & 1;
        ad = u32x4{0xddddu, kh * KV_REC + l * 16, kh * KV_REC + 1024 + (h * 18 + r) * 16, (kh + 2 * h) * KV_REC + 2176 + r * 8};
        PIN(i_m); PIN(i_t); PIN(wa0); PIN(wa1);
        asm volatile("" : "+{v[174:177]}"(ad));
      } else if (n == 7) {
        const int last = g & 1, u = 9 * xcd + 2 * (head & 3) + 2 * last;
        f2_kv = (r16 * 16 + (h ? 9 + ((18 * head + 17 * last) >> 7) : (u + 72) >> 4)) * 128;
        f2_q = ((2 * qb + h) * 16 + (u >> 4)) * 128;
        PIN(f2_kv); PIN(f2_q);
      } else if (n == 8) {
        PIN(par[0]);
        dt = bf_val(__builtin_bit_cast(u16, (__bf16)expf(bf_lo(par[0]))));
        PIN(dt);
      } else if (n == 9) {
        PIN(par[1]);
        lr = bf_lo(par[1]);
        mag = expf(dt * lr);
        PIN(mag);
      } else if (n == 10) {
        PIN(par[2]);
        li = bf_lo(par[2]);
        id = 2.f / (lr * lr + li * li);
        PIN(id);
      } else if (n == 11) {
        PIN(dt);
        sincosf(dt * li, &sn, &cs);
        PIN(sn); PIN(cs);
      } else if (n == 12) {
        PIN(mag);
        const float ar = mag * cs, ai = mag * sn, nr = ar - 1.f;
        cs = ar; sn = -ai;
        mag = (nr * lr + ai * li) * id; id *= ai * lr - nr * li;
        a16 = csq(csq(csq(csq(f32x2{ar, -ai}))));
        PIN(cs); PIN(sn); PIN(mag); PIN(id); PIN(a16);
      } else if (n == 14) {
        PIN(f0);
        f_own = (b & 2 ? (b & 1 ? f0[3] : f0[2]) : (b & 1 ? f0[1] : f0[0])) + 1;
        f1v = lw == 3 && l == b >> 2 ? WS_F1 + (b >> 2) * 128 + (b & 3) * 4 : WS_END;
        PIN(f_own); PIN(f1v);
      } else {
        PIN(a16);
        const f32x2 one = {1.f, 0.f}, a32 = csq(a16), a64 = csq(a32), a128 = csq(a64);
        const f32x2 s0 = cmul(lw & 1 ? a32 : one, lw & 2 ? a64 : one), s1 = cmul(s0, a16);
        LDS(f32x4) p = (LDS(f32x4))(size_t)(L_PAR + (lw >> 1) * L_SLOT + (lw & 1) * 4096 + l * 16);
        p[0] = f32x4{cs, sn, mag, id};
        p[64] = f32x4{a16[0], a16[1], a128[0], a128[1]};
        p[128] = f32x4{s0[0], s0[1], s1[0], s1[1]};
        const f32x2 a1024 = csq(csq(csq(a128)));
        PIN(ds);
        p[192] = f32x4{a1024[0], a1024[1], __uint_as_float(ds[0]), __uint_as_float(ds[1])};
        bad[f1v == WS_END ? 17 : 16] = f_own + 1;
        FENCE();
        __builtin_amdgcn_s_waitcnt(0xC07F);
      }
      SBAR();
    };
    piece(0); piece(1);
    dma4(k1(wp, 1, w_step), w0, w1, w2, w3, slot_of(1) + L_W + xd);
    RAW_LD("global_load_ushort %0, %1, %2", par[0], par_v, logdt);
    RAW_LD("global_load_ushort %0, %1, %2", par[1], par_v, lre);
    RAW_LD("global_load_ushort %0, %1, %2", par[2], par_v, lim);
    RAW_LD("global_load_dwordx2 %0, %1, %2", ds, ds_v, dskip + c3 * 144);
    f1_load(f0);
#pragma unroll
    for (int c = 2; c < 9; ++c) {
      dma4(k1(x1, c, 128), rows0, rows1, xo2, xo3, slot_of(c) + xd);
      if (c > 2) piece(c < 6 ? 2 * c - 4 : c + 2);
      if (c < 4) sync_dma4<16>(k1(wp, c, w_step), w0, w1, w2, w3, slot_of(c) + L_W + xd);
      else sync_dma4<12>(k1(wp, c, w_step), w0, w1, w2, w3, slot_of(c) + L_W + xd);
      if (c == 2) {
        asm volatile("" ::"v"(code_sink));
        if (lw == 3) {
          ST128((u32x4{}), cl_f);
          ST128((u32x4{}), cl_x);
          ST64((u32x2{EMPTY, EMPTY}), cl_a, WS_AG + cj * 4096 + (15 - rt) * 256);
        }
      } else if (c < 8) {
        piece(c < 6 ? 2 * c - 3 : 21 - c);
      }
    }
    if (two) {
      WAIT_VM(8); BAR();
      WAIT_VM(0); BAR();
      if (lw == 3) ST32(f_own, f1v, 0);
    } else {
      x_b(9);
      sync_dma2<12>(b2, rows0, rows1, slot_of(9) + bd);
      x_b(10); w_b(10);
      sync_dma4<12>(k2(x2, 11), rows0, rows1, xo2, xo3, slot_of(11) + xd);
      w_b(11); x_b(12); w_b(12);
      if (lw == 3) ST32(f_own, f1v, 0);
      sync_dma4<18>(k2(x2, 13), rows0, rows1, xo2, xo3, slot_of(13) + xd);
      w_b(13);
    }
    piece(11); piece(12); piece(13);
    if (two) {
      BAR();
      BAR();
      f1_load(f1);
      WAIT_VM(0);
    } else {
#pragma unroll
      for (int c = 14; c < 18; ++c) {
        sync_dma4<18>(k2(x2, c), rows0, rows1, xo2, xo3, slot_of(c) + xd);
        w_b(c);
        if (c == 15) f1_load(f1);
      }
      WAIT_VM(18); BAR();
      WAIT_VM(12);
    }
    if (lw == 3) {
#pragma unroll 1
      for (int spin = 0; spin < SPIN_MAX; ++spin) {
        PIN(f1);
        if (all_lanes(f1[0] != f0[0] && f1[1] != f0[1] && f1[2] != f0[2] && f1[3] != f0[3])) break;
        __builtin_amdgcn_s_sleep(4);
        asm volatile("global_load_dwordx4 %0, %1, %2 sc1\n s_waitcnt vmcnt(0)" : "+v"(f1) : "v"(lane * 128), "s"(f1p) : "memory");
      }
    }
    BAR();
    unsigned f2[2][2];
    auto f2_load = [&](int i) {
      asm volatile("global_load_dword %0, %2, %4 sc1\n global_load_dword %1, %3, %4 sc1"
                   : "=&v"(f2[i][0]), "=&v"(f2[i][1]) : "v"(f2_kv), "v"(f2_q), "s"(ws + WS_F2) : "memory");
    };
    if (two) __builtin_amdgcn_s_sleep(TWO_SLEEP);
    f2_load(0);
    WAIT_VM(8);
    if (!two) BAR();
    f2_load(1);
    WAIT_VM(4);
    if (!two) BAR();
    {
      unsigned spin;
      asm volatile("s_mov_b32 %4, %8\n"
                   "Lpoll_%=:\n s_waitcnt vmcnt(2)\n v_and_b32 %0, %0, %1\n v_cmp_ne_u32 vcc, 1, %0\n s_cbranch_vccz Lseen_%=\n"
                   "global_load_dword %0, %5, %7 sc1\n global_load_dword %1, %6, %7 sc1\n"
                   "s_waitcnt vmcnt(2)\n v_and_b32 %2, %2, %3\n v_cmp_ne_u32 vcc, 1, %2\n s_cbranch_vccz Lseen_%=\n"
                   "global_load_dword %2, %5, %7 sc1\n global_load_dword %3, %6, %7 sc1\n"
                   "s_sub_u32 %4, %4, 1\n s_cbranch_scc0 Lpoll_%=\n Lseen_%=:"
                   : "+v"(f2[0][0]), "+v"(f2[0][1]), "+v"(f2[1][0]), "+v"(f2[1][1]), "=&s"(spin)
                   : "v"(f2_kv), "v"(f2_q), "s"(ws + WS_F2), "n"(SPIN_MAX) : "vcc", "scc", "memory");
    }
    dev_dma4(kv + lw * 4096, lane * 16, lane * 16, lane * 16, lane * 16, lw * 4096);
    u32x16 q;
    u32x4 qr;
    {
      const unsigned qs = WS_Q + head * Q_HEAD + 8 * qb * Q_REC, q0 = qs + 2 * lw * Q_REC, q1 = qs + 2 * (lw ^ 1) * Q_REC;
#pragma unroll
      for (int i = 0; i < 4; ++i) {
        const u32x4 t = LD128(lane * 16 + (i & 1) * Q_REC, i < 2 ? q0 : q1);
        q[4 * i] = t[0]; q[4 * i + 1] = t[1]; q[4 * i + 2] = t[2]; q[4 * i + 3] = t[3];
      }
      const u32x2 t0 = LD64(qt, q0), t1 = LD64(qt, q1);
      qr = u32x4{t0[0], t0[1], t1[0], t1[1]};
    }
    dev_dma4(kv + 16384 + lw * 4096, lane * 16, lane * 16, lane * 16, lane * 16, 16384 + lw * 4096);
    {
      const int hq = head & 3;
      const bool z0 = hq == 1 && h == 0, z3 = hq == 2 && h == 1;
#pragma unroll
      for (int i = 0; i < 4; ++i) { q[4 * i] = z0 ? 0u : q[4 * i]; q[4 * i + 3] = z3 ? 0u : q[4 * i + 3]; }
      if (hq == 0) { qr[1] = 0; qr[3] = 0; }
      if (hq == 3) { qr[0] = 0; qr[2] = 0; }
    }
    asm volatile("" : "+{v[128:143]}"(q), "+{v[144:147]}"(qr));
    __builtin_amdgcn_s_setprio(2);
    asm volatile("s_waitcnt vmcnt(0)" ::"v"(f2[0][0]), "v"(f2[0][1]), "v"(f2[1][0]), "v"(f2[1][1]) : "memory");
    BAR();
    {
      unsigned it;
      asm volatile(ATTN_LOOP
                   : "+{v[56:63]}"(sz), "+{v[64:95]}"(oa), "+{v[96:127]}"(ob), "+{v[158:173]}"(vv), "+{v[174:177]}"(ad), [it] "=&s"(it)
                   : "{v[128:143]}"(q), "{v[144:147]}"(qr), [vm] "s"(0x0003ffff0003ffffull), [bm] "s"(0x51u)
                   : "v0", "v1", "v2", "v3", "v4", "v5", "v6", "v7", "v8", "v9", "v10", "v11", "v12", "v13", "v14", "v15", "v16", "v17",
                     "v18", "v19", "v20", "v21", "v22", "v23", "v24", "v25", "v26", "v27", "v28", "v29", "v30", "v31", "v32", "v33",
                     "v34", "v35", "v36", "v37", "v38", "v39", "v40", "v41", "v42", "v43", "v44", "v45", "v46", "v47", "v48", "v49",
                     "v50", "v51", "v52", "v53", "v54", "v55", "v148", "v149", "v150", "v151", "v152", "v153", "v154", "v155",
                     "v156", "v157", "scc", "memory");
    }
    {
      LDS(f32x4) pw = (LDS(f32x4))(size_t)x_w[0];
      pw[0] = f32x4{ob[0], ob[1], ob[2], ob[3]}; pw[64] = f32x4{ob[4], ob[5], ob[6], ob[7]};
      pw[128] = f32x4{ob[16], ob[17], ob[18], ob[19]};
      *(LDS(f32x4))(size_t)x_w[1] = f32x4{ob[20], ob[21], ob[22], ob[23]};
      *(LDS(f32x4))(size_t)x_w[2] = f32x4{ob[8], ob[9], ob[10], ob[26]};
      *(LDS(f32x2))(size_t)x_w[3] = f32x2{ob[24], ob[25]};
      SBAR();
      f32x2 z0 = {0.f, 0.f}, z1 = z0;
#pragma unroll
      for (int i = 0; i < 26; i += (i == 8 ? 8 : 2)) {
        z0 = __builtin_elementwise_fma(f32x2{oa[i], oa[i + 1]}, f32x2{0.f, 0.f}, z0);
        z1 = __builtin_elementwise_fma(f32x2{ob[i], ob[i + 1]}, f32x2{0.f, 0.f}, z1);
      }
      z0 += z1;
      const float lo = fminf(fminf(oa[10], oa[26]), fminf(ob[10], ob[26])), hi = oa[10] + oa[26] + ob[10] + ob[26];
      if (!(z0[0] + z0[1] == 0.f && lo >= 0x1p-64f && hi <= 0x1p64f)) *bad = 1;
    }
    BARRIER();
    auto store_o = [&](const f32x16& o0, const f32x16& o1) {
      ST128(pack8(o0, 0), o_m);
      ST128(pack8(o1, 0), o_m2);
      const u32x2 t = __builtin_amdgcn_permlane32_swap(pack_bf16(o0[8], o0[9]), pack_bf16(o1[8], o1[9]), false, false);
      ST32(t[0], o_t, WS_O + (8 * qb + 2 * lw) * O_TILE + 32768 + head * 128);
    };
    {
      const unsigned redo = *bad;
      LDS(const f32x4) p = (LDS(const f32x4))(size_t)x_r[0];
      f32x4 e = *(LDS(const f32x4))(size_t)x_r[2], a = p[0], bb = p[64], c = p[128], d = *(LDS(const f32x4))(size_t)x_r[1];
      f32x2 t = *(LDS(const f32x2))(size_t)x_r[3];
      SBAR();
      f32x16 o0, o1;
      if (__builtin_expect(__builtin_amdgcn_readfirstlane(redo), 0)) {
        reload_kv(kv, w, lane);
        attn_shifted(lane, __builtin_bit_cast(bf16x8, u32x4{q[0], q[1], q[2], q[3]}), __builtin_bit_cast(bf16x8, u32x4{q[4], q[5], q[6], q[7]}),
                     __builtin_bit_cast(s16x4, u32x2{qr[0], qr[1]}), vfill, o0, o1);
        o0 *= __builtin_amdgcn_rcpf(o0[10]); o1 *= __builtin_amdgcn_rcpf(o1[10]);
        store_o(o0, o1);
        BARRIER();
        BARRIER();
      } else {
        const float i0 = __builtin_amdgcn_rcpf(oa[10] + e[2]), i1 = __builtin_amdgcn_rcpf(oa[26] + e[3]);
#pragma unroll
        for (int i = 0; i < 10; ++i) {
          o0[i] = __builtin_fmaf(i < 4 ? a[i & 3] : i < 8 ? bb[i & 3] : e[i & 3], i0, oa[i] * i0);
          o1[i] = __builtin_fmaf(i < 4 ? c[i & 3] : i < 8 ? d[i & 3] : t[i & 1], i1, oa[16 + i] * i1);
        }
        store_o(o0, o1);
      }
    }
    FENCE();
    constexpr int PD = 16;
    bf16x8 wf[PD + 1];
    auto step = [](int n) { return n < 30 ? n / 5 : 6 + n % 3; };
    auto tile = [](int n) { return n < 30 ? n % 5 : n / 3 - 10; };
    auto load = [&](int n) {
      const lds_ptr p = (lds_ptr)(size_t)(step(n) < 8 ? wa0 : wa1) + step(n) % 8 * 4608 + tile(n) * 32;
      wf[n % (PD + 1)] = lds_frag_tr(p, p + 144);
    };
    auto preload = [&](int n0, int n1) {
#pragma unroll
      for (int n = n0; n < n1; ++n) load(n);
      SBAR();
    };
    ISSUE_C(-1, 3);
    ISSUE_C(63, 4);
    ISSUE_C(-1, 5);
    WAIT_VM(10);
    ST32(1u, f4_w, 0);
    ISSUE_C(63, 6);
    ISSUE_C(11, 7);
    preload(0, 5);
    ISSUE_C(11, 8);
    preload(5, 10);
    WAIT_VM(10); BAR();
    unsigned f4;
    RAW_LD("global_load_dword %0, %1, %2 sc1", f4, f4_r, ws);
    preload(10, PD);
    WAIT_VM(1); BAR();
#pragma unroll 1
    for (int spin = 0; spin < SPIN_MAX; ++spin) {
      asm volatile("s_waitcnt vmcnt(0)" : "+v"(f4)::"memory");
      if (all_lanes(f4 == 1u)) break;
      asm volatile("global_load_dword %0, %1, %2 sc1" : "+v"(f4) : "v"(f4_r), "s"(ws) : "memory");
    }
    WAIT_VM(0);
    u32x4 of[9][2];
    const unsigned so = WS_O + (2 * t3 + (lw & 1)) * O_TILE, sm = so + (lw >> 1) * 16384, st = so + 32768 + (lw >> 1) * 2048;
    auto load_o = [&](int i) { of[i >> 1][i & 1] = LD128(i_m + (i & 2) * 1024 + (i & 1) * 256, sm + (i >> 2) * 4096); };
#pragma unroll
    for (int i = 0; i < 16; ++i) {
      if (i < 8) load_o(i);
      else of[8][i >> 2 & 1][i & 3] = LD32(i_t + (i & 4) * 16 + (i & 3) * 128, st);
      SBAR();
    }
    f32x4 acc[2][5];
    unsigned y_done;
#pragma unroll
    for (int n = 0; n < 45; ++n) {
      SBAR();
      mma16(acc[0][tile(n)], wf[n % (PD + 1)], of[step(n)][0], n < 5);
      SBAR();
      if (n + PD < 45) load(n + PD);
      if (n == 29) y_done = bad[4 + (lane & 3)];
      if (n > 33 && n % 3) stage_tile(acc, st_w, n % 3 - 1, tile(n) - 1);
      SBAR();
      mma16(acc[1][tile(n)], wf[n % (PD + 1)], of[step(n)][1], n < 5);
      SBAR();
      if (n < 16 && !(n & 1)) load_o(8 + n / 2);
      if (n == 33 && __builtin_expect(!all_lanes(y_done), 0)) {
#pragma unroll 1
        for (int spin = 0; spin < SPIN_MAX && !all_lanes(bad[4 + (lane & 3)]); ++spin) {}
      }
      if (n == 33) FENCE();
    }
    SBAR();
    stage_done(acc, st_w4, bad + 8 + lw, bad + 8 + (lw ^ 2));
    f32x4 v[6];
#pragma unroll
    for (int i = 0; i < 6; ++i) v[i] = staged(st_rd, 0, i) + staged(st_rd, L_ST, i);
    store_rows(v, out + (64 * t3 + 32 * (lw & 1) + 16 * (lw >> 1)) * 4608 + c3 * 288, st_g);
  } else {
    BuTile bu;
    bu.init(w, lane);
    unsigned ga[4], gw[2];
    {
      const int q4 = (lane & 15) >> 2, p = lane & 3, gq = 4 * ((lane >> 4) & 1) + (cj >= 9 ? p : 2 * (p & 1) + (p >> 1));
#pragma unroll
      for (int ks = 0; ks < 4; ++ks) { ga[ks] = bu.ba[ks] + slot_base(0) - L_W + (w & 1) * 8192; PIN(ga[ks]); }
      gw[0] = slot_base(0) + L_W + (8 * h + q4) * 256 + (((2 * (w >> 1)) ^ q4) << 6) + (gq << 3);
      gw[1] = gw[0] ^ 64;
      PIN(gw[0]); PIN(gw[1]);
    }
    const unsigned lv = lane * 16;
    u32x2 xs[6], ds;
    f32x2 dm[2];
    LATER_BASES();
    unsigned f2 = WS_F2 + (rt * 16 + cj) * 128, one_v = threadIdx.x ? WS_END : 0;
    unsigned y_off = (64 * t3 + 32 * (w & 1) + 16 * (w >> 1)) * 1152, x_skip = y_off + 3456 + c3 * 144;
    y_off = 4 * y_off + 2304 + c3 * 288;
    asm volatile("" : "+s"(y_off), "+s"(x_skip), "+s"(f2));
    PIN(one_v);
    unsigned par_r, row2, ag_w, ag_r, xs_v, ds_v, t_w, t_r, h_vo, tv, tv2, f3v, wm[4], wr, ca0, ca1, st_w, st_w4, st_rd, st_g;
    unsigned cv[5] = {rows0, rows1, xo2, xo3};
    auto piece = [&](int n) __attribute__((always_inline)) {
      int l = lane;
      PIN(l);
      const int r = l & 31, h = l >> 5, g = l >> 4, r16 = l & 15;
      if (n == 0) {
        t_r = L_SPARE + w * 1024 + (l & 2) * 256 + (((8 * g + (r16 >> 2)) ^ (2 * (l & 2))) << 4) + (l & 1) * 8;
        PIN(t_r);
      } else if (n == 1) {
        t_w = L_SPARE + w * 1024 + h * 512 + ((r ^ (4 * h)) << 4);
        par_r = L_PAR + (w >> 1) * L_SLOT + (w & 1) * 4096 + r * 16;
        PIN(t_w); PIN(par_r);
      } else if (n == 2) {
        row2 = L_SPARE + (15 + 2 * w) * 256 + l * 8;
        ag_w = w == 3 && l >= 32 ? (14 - rt) * 256 + l * 8 : 4096;
        ag_r = (16 - rt + 7 * h) * 256 + l * 8;
        PIN(row2); PIN(ag_w); PIN(ag_r);
      } else if (n == 3) {
        const int j = __umul24(l, 57) >> 9;
#pragma unroll
        for (int i = 0; i < 4; ++i) { wm[i] = (j >> 1) * (1152 - 288) + (j & 1) * (9216 - 144) + l * 16 + i * 1728; PIN(wm[i]); }
        wr = wm[0] + (j & 1) * 28 * 1152;
        PIN(wr);
      } else if (n == 4) {
        tv = l * 128; tv2 = min(l, 35) * 128;
        PIN(tv); PIN(tv2);
      } else if (n == 5) {
        h_vo = WS_H + (8 * rt + 2 * w + (r16 >> 3)) * H_TILE + cj * 1024 + g * 256 + (l & 7) * 16;
        f3v = l <= two ? l * 256 : WS_END;
        cv[4] = rows0 + (64 - 32 * (w & 1)) * 1152;
        PIN(h_vo); PIN(f3v); PIN(cv[4]);
      } else if (n == 6) {
        ca0 = L_C + (w >> 1) * 9216 + r16 * 128 + ((g ^ (r16 >> 1)) << 4); ca1 = ca0 ^ 64;
        PIN(ca0); PIN(ca1);
      } else if (n == 7) {
        const int row3 = __umul24(l, 57) >> 10;
        ds_v = row3 * -144 + l * 8;
        xs_v = row3 * (1152 - 144) + l * 8;
        PIN(ds_v);
        st_g = l < 54 ? row3 * (4608 - 288) + l * 16 : 0x100000;
        PIN(xs_v); PIN(st_g);
      } else {
        st_w = L_STY + (w >> 1) * L_ST + (32 * (w & 1) + r16) * L_SROW + g * 16; st_w4 = st_w + 256 + h * (16 * L_SROW - 32);
        st_rd = L_STY + (32 * (w & 1) + 16 * (w >> 1)) * L_SROW + l * 16;
        PIN(st_w); PIN(st_w4); PIN(st_rd);
      }
    };
    auto hook_qkv = [&](int c) {
      piece(c);
    };
    Scan s1, s2;
    const int seg = 2 * w + h;
    const rsrc_t ag2 = __builtin_amdgcn_make_buffer_rsrc(ws + WS_AG + cj * 4096, 0, 4096, 0x00020000);
    const rsrc_t ag1 = __builtin_amdgcn_make_buffer_rsrc(ws + WS_AG + cj * 4096 + 8192, 0, 4096, 0x00020000);
    auto clear_rows = [&]() {
      *(LDS(f32x2))(size_t)(L_SPARE + (8 + min(seg, 6)) * 256 + r * 8) = f32x2{};
      bad[lane & 15] = 0;
    };
    const char *xr, *xr2;
    auto skip_base = [&]() {
      asm volatile("" : "+s"(x_skip));
      xr = xb + x_skip; xr2 = xr + 9 * 1152;
      asm volatile("" : "+s"(xr), "+s"(xr2));
    };
    auto skip_load = [&]() {
      RAW_LD("global_load_dwordx2 %0, %1, %2 offset:-3456", xs[0], xs_v, xr);
      RAW_LD("global_load_dwordx2 %0, %1, %2", xs[1], xs_v, xr);
      RAW_LD("global_load_dwordx2 %0, %1, %2 offset:3456", xs[2], xs_v, xr);
      RAW_LD("global_load_dwordx2 %0, %1, %2 offset:-3456", xs[3], xs_v, xr2);
      RAW_LD("global_load_dwordx2 %0, %1, %2", xs[4], xs_v, xr2);
      RAW_LD("global_load_dwordx2 %0, %1, %2 offset:3456", xs[5], ds_v, xr2);
    };
    auto params = [&]() {
      scan_params(s2, par_r, h);
      ds = *(LDS(const u32x2))((lds_ptr)(size_t)(par_r + h * 512) + 3080);
    };
    auto skip_wait = [&]() {
      asm volatile("s_waitcnt vmcnt(0)" : "+v"(xs[0]), "+v"(xs[1]), "+v"(xs[2]), "+v"(xs[3]), "+v"(xs[4]), "+v"(xs[5])::"memory");
      dm[0] = f32x2{bf_lo(ds[0]), bf_hi(ds[0])}; dm[1] = f32x2{bf_lo(ds[1]), bf_hi(ds[1])};
      PIN(dm[0]); PIN(dm[1]);
    };
    auto skip = [&](int n) {
      unsigned x = xs[n >> 1][n & 1];
      PIN(x);
      f32x2 t = {bf_lo(x), bf_hi(x)};
      t *= dm[n & 1];
      x = pack_bf16(t[0], t[1]);
      PIN(x);
      xs[n >> 1][n & 1] = x;
    };
    f32x16 re = {}, im = {};
    asm volatile("s_waitcnt vmcnt(0)" ::"v"(code_sink));
    unsigned v_sink;
    const char* v_lines = ws + WS_KV + 1024 + (4 * rt + w) * KV_REC;
    {
      const unsigned q9 = __umul24(lane, 57) >> 9;
      v_sink = min(7 * (cj - 9) + q9, 31u) * KV_HEAD + (lane - 9 * q9) * 128;
      asm volatile("" : "+v"(v_sink), "+s"(v_lines));
    }
    if (two) {
      unsigned sink;
      f32x16 re1 = {}, im1 = {};
      auto load = [&](int ks, int s) { bu.load(s, ks, 8192 * s); bu.f[1][3 * ks] = bu.f[0][3 * ks]; };
      auto half = [&](int k) {
#pragma unroll
        for (int i = 0; i < 4; ++i) {
          bu.mma(0, 2 * k + i, re, im);
          SBAR();
          load((k ^ 2) + (i >> 1), i & 1);
          SBAR();
          bu.mma(1, 2 * k + i, re1, im1);
        }
      };
      bu.move(slot_of(0));
      BAR();
#pragma unroll
      for (int i = 0; i < 4; ++i) load(i >> 1, i & 1);
      RAW_LD("global_load_dword %0, %1, %2", sink, min(lane, 62) * 128, (w < 2 ? bre : bim) + ((xcd & 1) * 32 + 2 * rt + (w & 1)) * 63 * 128);
#pragma unroll 1
      for (int c = 0; c < 9; ++c) {
        half(0);
        bu.move(c == 2 || c == 7 ? -4 * L_SLOT : L_SLOT);
        SBAR();
        if (c < 8) BARRIER();
        half(2);
        SBAR();
      }
      asm volatile("s_waitcnt vmcnt(0)" ::"v"(sink));
#pragma unroll
      for (int i = 0; i < 9; ++i) piece(i);
      BARRIER();
      params();
      scan_params(s1, par_r + 512, h);
      const unsigned row1 = L_SPARE + (7 - seg) * 256 + r * 8;
      scan_local(s1, re1, im1, row1);
      clear_rows();
      skip_base();
      skip_load();
      BARRIER();
      scan_tile<-1>(s1, ag1, row1, ag_w);
      skip_wait();
#pragma unroll
      for (int i = 0; i < 12; ++i) skip(i);
      BAR();
      scan_fetch(s1, ag1, ag_r);
      scan_finish(s1, rs, ag1, ag_r, t_w + 2 * L_SLOT - L_SPARE, t_r + 2 * L_SLOT - L_SPARE, h_vo + 2048);
    } else {
      const int tile0 = 4 * rt + 2 * (w & 1), u0 = 16 * cj + 8 * (w >> 1);
      const bool isq = u0 < 72;
      const unsigned rec = isq ? Q_REC : KV_REC;
      unsigned em[4], el[4] = {}, eh[4] = {}, lo = 0, hi = 0, hp = h;
      f32x16 acc[2][2] = {};
      if (cj < 9) {
        const unsigned hs = isq ? Q_HEAD : KV_HEAD, so = (isq ? WS_Q : WS_KV) + tile0 * rec, u4 = (isq ? u0 : u0 - 72) + 4;
        lo = 0x3c1e0u >> u4 % 9; hi = 0x30783u >> u4 % 9;
        unsigned sl = isq ? r : ((r & 19) | ((r & 4) << 1) | ((r & 8) >> 1)), m, t;
        gemm_qkv<false>(ga, gw, acc, hook_qkv, [&](int c) {
          const int js = c >> 1;
          if (c == 8) {
          } else if (!(c & 1)) {
            PIN(hp);
            const unsigned u = u4 + 2 * js + hp, q9 = __umul24(u, 57) >> 9, n = u - 4 - q9, hb = __umul24(n >> 1, hs) + so;
            m = u - 9 * q9;
            t = hb + rec - 256 + sl * 8;
            em[js] = m == 8 ? WS_END : hb + (n & 1) * 512 + sl * 16;
            PIN(m); PIN(t); PIN(em[js]);
          } else {
            PIN(m);
            m = 1u << m;
            PIN(m);
            el[js] = m & 0x140 ? t - hs : WS_END;
            eh[js] = m & 0x102 ? t + (m & 2 ? hs : 0) : WS_END;
            PIN(el[js]); PIN(eh[js]);
          }
        });
      } else {
        gemm_qkv<true>(ga, gw, acc, hook_qkv, [&](int c) {
          if (c == V_TOUCH) asm volatile("global_load_dword %0, %0, %1 sc1" : "+v"(v_sink) : "s"(v_lines) : "memory");
          if (c == 8) asm volatile("s_waitcnt vmcnt(0)" ::"v"(v_sink) : "memory");
          if (c < 2) {
            PIN(hp);
            const unsigned n = (cj < 13 ? 128 * (cj - 9) : 448) + 64 * (w >> 1) + 32 * c + r, hd = __umul24(n, 3641) >> 16;
            em[2 * c] = __umul24(hd, KV_HEAD - 288) + (hp * 18 + n) * 16 + (cj < 13 || w >= 2 ? WS_KV + 1024 + tile0 * KV_REC : WS_END);
            em[2 * c + 1] = em[2 * c] + 576;
            PIN(em[2 * c]); PIN(em[2 * c + 1]);
          }
        });
      }
      const float sc = isq ? 0.23570226039551584f * 1.4426950408889634f : 1.0f;
#pragma unroll
      for (int js = 0; js < 4; ++js) {
        if (js == JS9) {
          SBAR();
          BAR();
#pragma unroll
          for (int ks = 0; ks < 4; ++ks) bu.load(0, ks, slot_imm(9));
          SBAR();
        }
        const u32x4 v0 = pack8(acc[0][js >> 1], 8 * (js & 1), sc);
        ST128(v0, em[js]);
        const u32x4 v1 = pack8(acc[1][js >> 1], 8 * (js & 1), sc);
        ST128(v1, em[js] + rec);
        if (lo & (1 << 2 * js)) {
          ST64((u32x2{v0[0], v0[1]}), el[js], 0);
          ST64((u32x2{v1[0], v1[1]}), el[js], rec);
        }
        if (hi & (1 << 2 * js)) {
          ST64((u32x2{v0[2], v0[3]}), eh[js], 0);
          ST64((u32x2{v1[2], v1[3]}), eh[js], rec);
        }
      }
      clear_rows();
      bu.move(slot_base(10));
#pragma unroll
      for (int c = 9; c < 18; ++c) {
        const int s = (c - 9) & 1;
        SBAR();
        bu.mma(s, 0, re, im);
        SBAR();
        if (c < 17) {
          if (c - 9 == PUB_AT) {
            WAIT_VM(0);
          }
          BARRIER();
          if (c - 9 == PUB_AT) ST32(1u, one_v, f2);
        }
#pragma unroll
        for (int i = 1; i < 8; ++i) {
          SBAR();
          bu.mma(s, i, re, im);
          SBAR();
          if (c < 17 && i < 5) bu.load(s ^ 1, i - 1, slot_imm(c + 1));
          if (c < 16 && i > 5 && slot_base(c + 2) != slot_base(c + 1)) bu.move(slot_base(c + 2) - slot_base(c + 1), 2 * i - 12, 2 * i - 10);
          if (c == 12 && i == 5) skip_base();
          if (c == 13 && i == 5) skip_load();
          if (c == 14 && i == 5) params();
          if (c == 16 && i == 5) skip_wait();
          if (c == 16 && i > 5) { skip(2 * i - 12); skip(2 * i - 11); }
          if (c == 17) skip(i + 3);
          if (c == 17 && i == 7) skip(11);
        }
      }
      SBAR();
    }
    scan_local(s2, re, im, row2);
    FENCE();
    bad[12 + w] = 1;
#pragma unroll 1
    for (int spin = 0; spin < SPIN_MAX && !all_lanes(bad[12 + (lane & 3)]); ++spin) {}
    FENCE();
    scan_tile<1>(s2, ag2, row2, ag_w);
    __builtin_amdgcn_s_setprio(3);
    BARRIER();
    auto kv_rest = [&](int i) { dev_dma4(kv + 32768 + (4 * i + w) * 4096, lv, lv, lv, lv, 32768 + (4 * i + w) * 4096); };
#pragma unroll
    for (int i = 0; i < 5; ++i) kv_rest(i);
    scan_fetch(s2, ag2, ag_r);
    WAIT_VM(8);
    BAR();
    const unsigned t_b = __builtin_amdgcn_s_memtime();
#pragma unroll
    for (int i = 0; i < 8; ++i) PIN(s2.ag[i]);
    kv_rest(5); kv_rest(6);
    dev_dma2(kv + 147456 + w * 2048, lv, lv, 147456 + w * 2048);
    ST32(bad[16], one_v, WS_F1 + (b >> 2) * 128 + (b & 3) * 4);
    if (b < 243) touch((b < 81 ? wo : b < 162 ? cre - 81 * 8192 : cim - 162 * 8192) + (4 * b + w) * 2048, tv & 15 * 128);
    if (c3 & 3) touch(out + (64 * t3 + 16 * w) * 4608 + c3 * 288, (tv & 31 * 128) * 18);
    scan_finish(s2, rs, ag2, ag_r, t_w, t_r, h_vo);
    WAIT_VM(0);
    ST32(1u, f3v, WS_F3 + ((4 * rt + w) * 18 + cj) * 128);
    {
      const char* cs = (w < 2 ? cre : cim) + c3 * 72 * 1152 + (2 * (sb & 3) + (w & 1)) * 81 * 128;
      touch(cs, tv);
      touch(cs + 64 * 128, min(tv, 16u * 128));
    }
    auto issue_wo = [&]() {
      const char* src = wo + c3 * 144 + w * 144 * 1152;
      if (lane < 36) {
#pragma unroll 1
        for (int i = 0; i < 8; ++i) dma4_rows(src + i * 18 * 1152, wm, (8 * w + i) * 2304);
#pragma unroll
        for (int i = 0; i < 4; ++i) dma1(src + (18 * (4 * (i >> 1) + (i & 1)) + 16) * 1152, wr, 73728 + (4 * w + i) * 576);
      }
    };
    auto h_wait = [&](unsigned limit) {
      bool seen = false;
#pragma unroll 1
      for (int spin = 0; spin < SPIN_MAX; ++spin) {
        FENCE();
        seen = all_lanes(LD32(tv2, WS_F3 + t3 * 36 * 128) == 1u);
        if (seen || (unsigned)__builtin_amdgcn_s_memtime() - t_b > limit) break;
        __builtin_amdgcn_s_sleep(H_SLEEP);
      }
      FENCE();
      return seen;
    };
    const bool h_seen = h_wait(H_LIMIT);
    WAIT_VM(0);
    if (h_seen) touch(ws + WS_H + 4 * t3 * H_TILE + (4 * qb + w) * 36 * 128, tv2);
    BAR();
    issue_wo();
    if (!h_seen) h_wait(~0u);
    u32x4 hf[18][2];
    {
      const unsigned so = WS_H + (4 * t3 + 2 * (w & 1)) * H_TILE + (w >> 1) * 18432;
#pragma unroll
      for (int i = 0; i < 36; ++i) hf[i % 18][i / 18] = LD128(lv + (i % 18 & 3) * 1024, so + (i / 18) * H_TILE + (i % 18 >> 2) * 4096);
    }
    ISSUE_C(63, 0);
    ISSUE_C(-1, 1);
#pragma unroll
    for (int i = 0; i < 36; ++i) PIN(hf[i >> 1][i & 1]);
    ISSUE_C(0, 2);
    if (__builtin_expect(__builtin_amdgcn_readfirstlane(*bad), 0)) {
      reload_kv(kv, w, lane);
      BARRIER();
      issue_wo();
#pragma unroll 1
      for (int i = 0; i < 3; ++i) ISSUE_C(-1, i);
      WAIT_VM(0);
      BARRIER();
    }
    __builtin_amdgcn_s_setprio(1);
    constexpr int CD = 4, CR = CD + 4;
    bf16x8 cf[CR];
    auto step = [](int n) { return n < 80 ? n / 5 : 16 + n % 2; };
    auto tile = [](int n) { return n < 80 ? n % 5 : n / 2 - 40; };
    auto load = [&](int n) { cf[n % CR] = lds_frag((lds_ptr)(size_t)(step(n) & 1 ? ca1 : ca0) + c_slot(n / 10) + tile(n) * 2048); };
#pragma unroll
    for (int n = 0; n < CD + 3; ++n) load(n);
    f32x4 acc[2][5];
    expand<0, 90>([&](auto i) __attribute__((always_inline)) {
      constexpr int n = i.value, m = (n + CD) % 10, n0 = n + CD - m;
      SBAR();
      mma16(acc[0][tile(n)], cf[n % CR], hf[step(n)][0], n < 5);
      SBAR();
      if (m == 0 && n0 >= 20 && n0 <= 70) {
        if (n0 == 20) WAIT_VM(0);
        BARRIER();
        if (n0 == 20) __builtin_amdgcn_s_setprio(3);
      }
      if (n0 < 90 && m < 7) load(n0 + (m < 3 ? 2 * m : m + 3));
      SBAR();
      mma16(acc[1][tile(n)], cf[n % CR], hf[step(n)][1], n < 5);
      SBAR();
      if (n0 < 90 && m < 3) load(n0 + 2 * m + 1);
      if (n >= 82) stage_tile(acc, st_w, n & 1, tile(n) - 1);
    });
    SBAR();
    stage_done(acc, st_w4, bad + 4 + w, bad + 4 + (w ^ 2));
    f32x4 v[6];
#pragma unroll
    for (int i = 0; i < 6; ++i)
      v[i] = staged(st_rd, 0, i) + staged(st_rd, L_ST, i) + f32x4{bf_lo(xs[i][0]), bf_hi(xs[i][0]), bf_lo(xs[i][1]), bf_hi(xs[i][1])};
    asm volatile("Lcode_end:");
    store_rows(v, out + y_off, st_g);
  }
}

extern "C" void kernel_launch(void* const* d_in, const int* in_sizes, int n_in, void* d_out, int out_size,
                              void* d_ws, size_t ws_size, hipStream_t stream) {
  (void)hipFuncSetAttribute((const void*)k_fused, hipFuncAttributeMaxDynamicSharedMemorySize, L_ALL);
  k_fused<<<256, 512, L_ALL, stream>>>((const char*)d_in[0], (const char*)d_in[1], (const char*)d_in[2], (const char*)d_in[3],
                                       (const char*)d_in[4], (const char*)d_in[5], (const char*)d_in[6], (const char*)d_in[7],
                                       (const char*)d_in[8], (const char*)d_in[9], (const char*)d_in[10], (const char*)d_in[11],
                                       (char*)d_out, (char*)d_ws);
}
```
